```python
import torch

def _softmax(x, axis=-1):
    return torch.softmax(x, dim=axis)
def _gelu(x):
    return torch.nn.functional.gelu(x, approximate="tanh")
def _silu(x):
    return torch.nn.functional.silu(x)
def _sigmoid(x):
    return torch.sigmoid(x)
def _layernorm(x, axis=-1, eps=1e-5):
    m = x.mean(dim=axis, keepdim=True)
    v = x.var(dim=axis, keepdim=True, unbiased=False)
    return (x - m) / torch.sqrt(v + eps)
def _scatter_add(tgt, idx, src, axis=0):
    return tgt.clone().index_add_(axis, idx, src)
def _segment_sum(src, seg, n):
    out = torch.zeros((n,) + src.shape[1:], dtype=src.dtype)
    return out.index_add_(0, seg, src)
def _pad(x, pw):
    flat = tuple(v for pair in reversed(pw) for v in pair)
    return torch.nn.functional.pad(x, flat)

kernel_name = 'concat_heads__ssm_conv_h3__linear_kernel_hedgehog__prompt'

def setup_inputs():

    _w = (lambda a: (lambda t: t.to(torch.bfloat16) if t.is_floating_point() else t)(torch.as_tensor(a)))
    def _A_setup():
        B, S, D = 1, 2048, 1024
        ks = 4
        return {
            "q": _w((lambda *s: torch.randn(*s, dtype=torch.bfloat16) * 0.05)(B, S, D)),
            "k": _w((lambda *s: torch.randn(*s, dtype=torch.bfloat16) * 0.8)(B, S, D)),
            "v": _w((lambda *s: torch.randn(*s, dtype=torch.bfloat16) * 0.8)(B, S, D)),
            "shift_k": _w((lambda *s: torch.randn(*s, dtype=torch.bfloat16) * 0.8)(D, ks) * 0.5),
            "ssm_k": _w((lambda *s: torch.randn(*s, dtype=torch.bfloat16) * 0.8)(D, S) * (1.0 / S ** 0.5)),
            "ssm_D": _w((lambda *s: torch.randn(*s, dtype=torch.bfloat16) * 0.8)(D)),
        }
    _A_in = _A_setup()

    def _B_setup():
        B, S, D, H = 1, 2048, 1024, 32
        Dh = D // H
        return {
            "x": _w((lambda *s: torch.randn(*s, dtype=torch.bfloat16) * 0.4)(B, S, D)),
            "wqkv": _w((lambda *s: torch.randn(*s, dtype=torch.bfloat16) * 0.4)(D, 3 * D)),
            "w_phi": _w((lambda *s: torch.randn(*s, dtype=torch.bfloat16) * 0.4)(H, Dh, Dh) * (Dh ** -0.5)),
            "wo": _w((lambda *s: torch.randn(*s, dtype=torch.bfloat16) * 0.05)(D, D)),
        }
    _B_in = _B_setup()

    return {
        "A_q": _A_in["q"],
        "A_k": _A_in["k"],
        "A_v": _A_in["v"],
        "A_shift_k": _A_in["shift_k"],
        "A_ssm_k": _A_in["ssm_k"],
        "A_ssm_D": _A_in["ssm_D"],
        "B_x": _B_in["x"],
        "B_wqkv": _B_in["wqkv"],
        "B_w_phi": _B_in["w_phi"],
        "B_wo": _B_in["wo"],
    }

def reference(A_q, A_k, A_v, A_shift_k, A_ssm_k, A_ssm_D, B_x, B_wqkv, B_w_phi, B_wo):
    def _A_ref(q, k, v, shift_k, ssm_k, ssm_D):
        B, S, D = q.shape
        ks = shift_k.shape[-1]
        k_pad = _pad(k, ((0, 0), (ks - 1, 0), (0, 0)))
        k_shift = torch.zeros_like(k)
        for t in range(ks):
            k_shift = k_shift + k_pad[:, t:t + S, :] * shift_k[:, ks - 1 - t][None, None, :]
        kv = k_shift * v
        L = 2 * S
        kv_pad = _pad(kv, ((0, 0), (0, S), (0, 0)))
        kf_pad = _pad(ssm_k, ((0, 0), (0, S)))
        kv_f = (lambda x, axes=(-2,-1): torch.fft.rfft2(x.float(), dim=axes))(kv_pad[:, None, :, :], axes=(1, 2))
        kf_f = (lambda x, axes=(-2,-1): torch.fft.rfft2(x.float(), dim=axes))(kf_pad[:, None, :], axes=(1, 2))
        kf_f = (lambda x, *d: x.permute(*d))(kf_f, 1, 2, 0)
        y_f = kv_f * kf_f[None, :, :, :]
        y = (lambda x, s, axes=(-2,-1): torch.fft.irfft2(x, s=s, dim=axes).to(torch.bfloat16))(y_f, (1, L), axes=(1, 2))[:, 0, :S, :]
        y = y + kv * ssm_D[None, None, :]
        return q * y

    def _B_ref(x, wqkv, w_phi, wo):
        B, S, D = x.shape
        H = 32; Dh = D // H
        qkv = (lambda x, *d: x.permute(*d))((x @ wqkv).reshape(B, S, 3, H, Dh), 2, 0, 3, 1, 4)
        q, k, v = qkv[0], qkv[1], qkv[2]
        pq = torch.einsum("bhsd,hdf->bhsf", q, w_phi)
        pk = torch.einsum("bhsd,hdf->bhsf", k, w_phi)
        phi_q = torch.concatenate([_softmax(pq, axis=-1), _softmax(-pq, axis=-1)], axis=-1)
        phi_k = torch.concatenate([_softmax(pk, axis=-1), _softmax(-pk, axis=-1)], axis=-1)
        kv = torch.einsum("bhsf,bhsd->bhfd", phi_k, v)
        z  = torch.einsum("bhsf->bhf", phi_k)
        num = torch.einsum("bhqf,bhfd->bhqd", phi_q, kv)
        den = torch.einsum("bhqf,bhf->bhq", phi_q, z)[..., None] + 1e-6
        o = num / den
        return (lambda x, *d: x.permute(*d))(o, 0, 2, 1, 3).reshape(B, S, D) @ wo

    a = _A_ref(A_q, A_k, A_v, A_shift_k, A_ssm_k, A_ssm_D)
    b = _B_ref(B_x, B_wqkv, B_w_phi, B_wo)
    return torch.concatenate([a, b], axis=-1)
```

```cpp
#include <hip/hip_runtime.h>
#include <hip/hip_bf16.h>
typedef __hip_bfloat16 bf16;
#define S_ 2048
#define D_ 1024
__device__ __forceinline__ float b2f(bf16 x){return __bfloat162float(x);}
__device__ __forceinline__ float rb(float x){return __bfloat162float(__float2bfloat16(x));}

__global__ void k_kv(const bf16* k,const bf16* v,const bf16* sk,bf16* kv){
  int i=blockIdx.x*blockDim.x+threadIdx.x; int s=i/D_, d=i%D_;
  float acc=0.f;
  for(int t=0;t<4;t++){ int ss=s+t-3; float kk= ss>=0? b2f(k[ss*D_+d]):0.f; acc=rb(acc+rb(kk*b2f(sk[d*4+3-t]))); }
  kv[i]=__float2bfloat16(acc*b2f(v[i]));
}
__global__ void k_conv(const bf16* q,const bf16* kv,const bf16* h,const bf16* Dv,bf16* out){
  int i=blockIdx.x*blockDim.x+threadIdx.x; int s=i/D_, d=i%D_;
  float acc=0.f;
  for(int j=0;j<=s;j++) acc+= b2f(kv[(s-j)*D_+d])*b2f(h[d*S_+j]);
  float y=rb(acc); y=rb(y+rb(b2f(kv[i])*b2f(Dv[d])));
  out[s*2048+d]=__float2bfloat16(b2f(q[i])*y);
}
__global__ void k_gemm(const bf16* A,const bf16* B,bf16* C,int M,int N,int K,int ldc){
  int n=blockIdx.x*blockDim.x+threadIdx.x; int m=blockIdx.y;
  float acc=0.f;
  for(int k=0;k<K;k++) acc+=b2f(A[m*K+k])*b2f(B[k*N+n]);
  C[m*ldc+n]=__float2bfloat16(acc);
}
__global__ __launch_bounds__(64,1) void k_phi(const bf16* qkv,const bf16* wphi,bf16* phi){
  int i=blockIdx.x*blockDim.x+threadIdx.x;
  int s=i/64, which=(i/32)&1, h=i&31;
  float p[32]; float mx=-1e30f, mn=1e30f;
  #pragma unroll
  for(int f=0;f<32;f++){ float a=0.f; _Pragma("unroll 1") for(int d=0;d<32;d++) a+=b2f(qkv[s*3072+which*1024+h*32+d])*b2f(wphi[h*1024+d*32+f]); a=rb(a); p[f]=a; mx=fmaxf(mx,a); mn=fminf(mn,a);}
  float s1=0,s2=0;
  #pragma unroll
  for(int f=0;f<32;f++){ s1+=__expf(p[f]-mx); s2+=__expf(mn-p[f]); }
  bf16* o=phi+((size_t)which*S_*32+s*32+h)*64;
  #pragma unroll
  for(int f=0;f<32;f++){ o[f]=__float2bfloat16(__expf(p[f]-mx)/s1); o[32+f]=__float2bfloat16(__expf(mn-p[f])/s2);}
}
__global__ void k_kvs(const bf16* phik,const bf16* qkv,bf16* kvs){
  int i=blockIdx.x*blockDim.x+threadIdx.x; if(i>=32*64*33) return; int h=i/(64*33), f=(i/33)%64, d=i%33;
  float a=0.f;
  for(int s=0;s<S_;s++){ float p=b2f(phik[(s*32+h)*64+f]); a+= d<32? p*b2f(qkv[s*3072+2048+h*32+d]) : p; }
  kvs[i]=__float2bfloat16(a);
}
__global__ void k_o(const bf16* phiq,const bf16* kvs,bf16* o){
  int i=blockIdx.x*blockDim.x+threadIdx.x; int s=i/1024,h=(i/32)&31,d=i&31;
  float n=0,dn=0;
  for(int f=0;f<64;f++){ float p=b2f(phiq[(s*32+h)*64+f]); n+=p*b2f(kvs[(h*64+f)*33+d]); dn+=p*b2f(kvs[(h*64+f)*33+32]); }
  n=rb(n); dn=rb(rb(dn)+1e-6f);
  o[i]=__float2bfloat16(n/dn);
}
extern "C" void kernel_launch(void* const* d_in, const int* in_sizes, int n_in, void* d_out, int out_size, void* d_ws, size_t ws_size, hipStream_t stream) {
  const bf16* q=(const bf16*)d_in[0],*k=(const bf16*)d_in[1],*v=(const bf16*)d_in[2],*sk=(const bf16*)d_in[3],*h=(const bf16*)d_in[4],*Dv=(const bf16*)d_in[5];
  const bf16* x=(const bf16*)d_in[6],*wqkv=(const bf16*)d_in[7],*wphi=(const bf16*)d_in[8],*wo=(const bf16*)d_in[9];
  bf16* out=(bf16*)d_out; char* ws=(char*)d_ws;
  bf16* kv=(bf16*)ws; bf16* qkv=(bf16*)(ws+(8<<20)); bf16* phi=(bf16*)(ws+(24<<20)); bf16* kvs=(bf16*)(ws+(48<<20)); bf16* o=(bf16*)(ws+(52<<20));
  k_kv<<<S_*D_/256,256,0,stream>>>(k,v,sk,kv);
  k_conv<<<S_*D_/256,256,0,stream>>>(q,kv,h,Dv,out);
  k_gemm<<<dim3(3072/256,2048),256,0,stream>>>(x,wqkv,qkv,2048,3072,1024,3072);
  k_phi<<<S_*64/64,64,0,stream>>>(qkv,wphi,phi);
  k_kvs<<<(32*64*33+255)/256,256,0,stream>>>(phi+(size_t)S_*32*64,qkv,kvs);
  k_o<<<S_*1024/256,256,0,stream>>>(phi,kvs,o);
  k_gemm<<<dim3(1024/256,2048),256,0,stream>>>(o,wo,out+1024,2048,1024,1024,2048);
}
```

```cpp
#include <hip/hip_runtime.h>
#include <stdint.h>

typedef short bf16x8 __attribute__((ext_vector_type(8)));
typedef short s16x4 __attribute__((ext_vector_type(4)));
typedef float f32x4 __attribute__((ext_vector_type(4)));
typedef float f32x2 __attribute__((ext_vector_type(2)));
typedef float f32x16 __attribute__((ext_vector_type(16)));
typedef unsigned u32x4 __attribute__((ext_vector_type(4)));
typedef unsigned u32x2 __attribute__((ext_vector_type(2)));
typedef __bf16 bfx2 __attribute__((ext_vector_type(2)));
typedef unsigned short u16;
#define DI __device__ __forceinline__
#define LAS __attribute__((address_space(3)))

constexpr int S_ = 2048, D_ = 1024;
constexpr size_t WS_XT = 0;
constexpr size_t WS_YT = 4u << 20;
constexpr size_t WS_QF = 8u << 20;
constexpr size_t WS_OF = 12u << 20;
constexpr size_t WS_KVP = 16u << 20;
constexpr size_t WS_ZP = 20u << 20;

DI unsigned pk_bf16(float lo, float hi) { f32x2 p = {lo, hi}; return __builtin_bit_cast(unsigned, __builtin_convertvector(p, bfx2)); }
DI float bf_lo(unsigned u) { return __uint_as_float(u << 16); }
DI float bf_hi(unsigned u) { return __uint_as_float(u & 0xffff0000u); }
DI float rbf(float x) { return bf_lo(pk_bf16(x, 0.f)); }
DI f32x16 mfma32(bf16x8 a, bf16x8 b, f32x16 c) { return __builtin_amdgcn_mfma_f32_32x32x16_bf16(a, b, c, 0, 0, 0); }
DI f32x4 mfma16(bf16x8 a, bf16x8 b, f32x4 c) { return __builtin_amdgcn_mfma_f32_16x16x32_bf16(a, b, c, 0, 0, 0); }
DI s16x4 tr_read(const char* p) { return __builtin_amdgcn_ds_read_tr16_b64_v4i16((LAS s16x4*)p); }
DI bf16x8 cat8(s16x4 a, s16x4 b) { return __builtin_shufflevector(a, b, 0, 1, 2, 3, 4, 5, 6, 7); }
DI void glds16(const void* g, void* l) { __builtin_amdgcn_global_load_lds((const unsigned*)g, (unsigned*)l, 16, 0, 0); }
#define WAIT_V(n) asm volatile("s_waitcnt vmcnt(%0)" ::"n"(n) : "memory")
#define WAIT_L0() asm volatile("s_waitcnt lgkmcnt(0)" ::: "memory")
#define BAR() do { WAIT_L0(); __builtin_amdgcn_s_barrier(); asm volatile("" ::: "memory"); } while (0)
DI float xhalf(float v) { return __shfl_xor(v, 32); }
DI bf16x8 pack8(const f32x16& x, int s) {
  u32x4 p;
  p[0] = pk_bf16(x[8 * s + 0], x[8 * s + 1]); p[1] = pk_bf16(x[8 * s + 2], x[8 * s + 3]);
  p[2] = pk_bf16(x[8 * s + 4], x[8 * s + 5]); p[3] = pk_bf16(x[8 * s + 6], x[8 * s + 7]);
  return __builtin_bit_cast(bf16x8, p);
}
DI void dual_softmax(const f32x16& pin, bf16x8* pos, bf16x8* neg) {
  float p[16];
#pragma unroll
  for (int i = 0; i < 8; ++i) { unsigned u = pk_bf16(pin[2 * i], pin[2 * i + 1]); p[2 * i] = bf_lo(u); p[2 * i + 1] = bf_hi(u); }
  float mx = p[0], mn = p[0];
#pragma unroll
  for (int i = 1; i < 16; ++i) { mx = fmaxf(mx, p[i]); mn = fminf(mn, p[i]); }
  mx = fmaxf(mx, xhalf(mx)); mn = fminf(mn, xhalf(mn));
  const float L2E = 1.4426950408889634f;
  float ep[16], en[16], sp = 0.f, sn = 0.f;
  const float mxl = mx * L2E, mnl = mn * L2E;
#pragma unroll
  for (int i = 0; i < 16; ++i) {
    ep[i] = __builtin_amdgcn_exp2f(fmaf(p[i], L2E, -mxl)); en[i] = __builtin_amdgcn_exp2f(fmaf(p[i], -L2E, mnl));
    sp += ep[i]; sn += en[i];
  }
  sp += xhalf(sp); sn += xhalf(sn);
  const float rp = __builtin_amdgcn_rcpf(sp), rn = __builtin_amdgcn_rcpf(sn);
#pragma unroll
  for (int s = 0; s < 2; ++s) {
    u32x4 a, b;
#pragma unroll
    for (int i = 0; i < 4; ++i) {
      a[i] = pk_bf16(ep[8 * s + 2 * i] * rp, ep[8 * s + 2 * i + 1] * rp);
      b[i] = pk_bf16(en[8 * s + 2 * i] * rn, en[8 * s + 2 * i + 1] * rn);
    }
    pos[s] = __builtin_bit_cast(bf16x8, a); neg[s] = __builtin_bit_cast(bf16x8, b);
  }
}

DI void role_a1(int tile, char* sm, const u16* __restrict__ k, const u16* __restrict__ v, const u16* __restrict__ shk, char* __restrict__ ws) {
  const int tid = threadIdx.x, cg = tile & 15, rb = tile >> 4;
  const int cgp = tid & 7, rg = tid >> 3;
  const int ch0 = cg * 64 + cgp * 8, r0 = rb * 128 + rg * 4;
  u32x4 kk[7], vv[4], sk[4];
#pragma unroll
  for (int i = 0; i < 7; ++i) {
    int r = r0 - 3 + i;
    u32x4 z = {0u, 0u, 0u, 0u};
    kk[i] = r >= 0 ? *(const u32x4*)(k + (size_t)r * D_ + ch0) : z;
  }
#pragma unroll
  for (int i = 0; i < 4; ++i) vv[i] = *(const u32x4*)(v + (size_t)(r0 + i) * D_ + ch0);
#pragma unroll
  for (int i = 0; i < 4; ++i) sk[i] = *(const u32x4*)(shk + ch0 * 4 + i * 8);
#pragma unroll
  for (int e = 0; e < 8; ++e) {
    const unsigned w01 = sk[e >> 1][(e & 1) * 2], w23 = sk[e >> 1][(e & 1) * 2 + 1];
    const float w[4] = {bf_lo(w01), bf_hi(w01), bf_lo(w23), bf_hi(w23)};
    float kf[7];
#pragma unroll
    for (int i = 0; i < 7; ++i) kf[i] = (e & 1) ? bf_hi(kk[i][e >> 1]) : bf_lo(kk[i][e >> 1]);
    float o[4];
#pragma unroll
    for (int i = 0; i < 4; ++i) {
      float a = rbf(kf[i] * w[3]);
      a = rbf(a + rbf(kf[i + 1] * w[2]));
      a = rbf(a + rbf(kf[i + 2] * w[1]));
      a = rbf(a + rbf(kf[i + 3] * w[0]));
      const float vf = (e & 1) ? bf_hi(vv[i][e >> 1]) : bf_lo(vv[i][e >> 1]);
      o[i] = a * vf;
    }
    u32x2 w2 = {pk_bf16(o[0], o[1]), pk_bf16(o[2], o[3])};
    *(u32x2*)(sm + (cgp * 8 + e) * 256 + ((rg ^ (2 * cgp)) << 3)) = w2;
  }
  __syncthreads();
#pragma unroll
  for (int it = 0; it < 4; ++it) {
    const int ch = it * 16 + (tid >> 4), l = tid & 15;
    const int c = ((l & 7) << 1) | (l >> 3);
    u32x4 d = *(const u32x4*)(sm + ch * 256 + ((c ^ (ch >> 3)) << 4));
    const int gc = rb * 16 + c;
    *(u32x4*)(ws + WS_XT + (size_t)(cg * 64 + ch) * 4096 + (gc & 1) * 2048 + (gc >> 1) * 16) = d;
  }
}

constexpr int G1_STAGE = 40960, G1_NBUF = 3, G1_WPHI = G1_STAGE * G1_NBUF;
constexpr int G1_LDS = G1_WPHI + 4096;

DI void role_gemm1(int bid, char* sm, const u16* __restrict__ x, const u16* __restrict__ W, const u16* __restrict__ wphi, char* __restrict__ ws) {
  const int tid = threadIdx.x, lane = tid & 63;
  const int wave = __builtin_amdgcn_readfirstlane(tid >> 6);
  const int xcd = bid & 7, idx = bid >> 3;
  const int rb = (xcd & 1) * 8 + (idx & 7), j = (xcd >> 1) * 4 + (idx >> 3);
  const int wm = wave & 1, wn = wave >> 1;
  const int h = lane >> 5, l31 = lane & 31;
  const u16* xs[4]; const u16* wsrc[6];
#pragma unroll
  for (int i = 0; i < 4; ++i) {
    const int p = wave * 4 + i, row = p * 8 + (lane >> 3), ch = (lane & 7) ^ ((row >> 1) & 7);
    xs[i] = x + (size_t)(rb * 128 + row) * 1024 + ch * 8;
  }
#pragma unroll
  for (int i = 0; i < 6; ++i) {
    const int p = wave * 6 + i, kg = p / 3, seg = p % 3;
    wsrc[i] = W + (size_t)(kg * 8 + ((lane >> 2) & 7)) * 3072 + seg * 1024 + j * 64 + (lane >> 5) * 32 + (lane & 3) * 8;
  }
  auto stage = [&](int t, int buf) {
    char* b = sm + buf * G1_STAGE;
#pragma unroll
    for (int i = 0; i < 4; ++i) glds16(xs[i] + t * 64, b + (wave * 4 + i) * 1024);
#pragma unroll
    for (int i = 0; i < 6; ++i) glds16(wsrc[i] + (size_t)t * 64 * 3072, b + 16384 + (wave * 6 + i) * 1024);
  };
  glds16(wphi + j * 2048 + wave * 512 + lane * 8, sm + G1_WPHI + wave * 1024);
  stage(0, 0); stage(1, 1); stage(2, 2);
  int xoff[4];
  {
    const int row = wm * 64 + l31, sw = (row >> 1) & 7;
#pragma unroll
    for (int s = 0; s < 4; ++s) xoff[s] = row * 128 + (((2 * s + h) ^ sw) << 4);
  }
  const int blk = (lane >> 4) & 1, q4 = (lane & 15) >> 2, p4 = lane & 3;
  const int woff = 16384 + h * 3072 + wn * 512 + q4 * 64 + (2 * blk + (p4 >> 1)) * 16 + (p4 & 1) * 8;
  f32x16 acc[2][3];
#pragma unroll
  for (int a = 0; a < 2; ++a)
#pragma unroll
    for (int b = 0; b < 3; ++b)
#pragma unroll
      for (int i = 0; i < 16; ++i) acc[a][b][i] = 0.f;
  bf16x8 xf[2][2], wf[2][3];
  auto rd = [&](int set, const char* b, int s) {
#pragma unroll
    for (int ts = 0; ts < 2; ++ts) xf[set][ts] = *(const bf16x8*)(b + xoff[s] + ts * 4096);
#pragma unroll
    for (int g = 0; g < 3; ++g) wf[set][g] = cat8(tr_read(b + woff + s * 6144 + g * 1024), tr_read(b + woff + s * 6144 + g * 1024 + 256));
  };
  auto mm = [&](int set) {
#pragma unroll
    for (int ts = 0; ts < 2; ++ts) {
      acc[ts][0] = mfma32(wf[set][0], xf[set][ts], acc[ts][0]);
      acc[ts][1] = mfma32(wf[set][1], xf[set][ts], acc[ts][1]);
      acc[ts][2] = mfma32(xf[set][ts], wf[set][2], acc[ts][2]);
    }
  };
  WAIT_V(20); BAR();
  rd(0, sm, 0);
  int cur = 0;
#pragma unroll 1
  for (int t = 0; t < 16; ++t) {
    const char* b = sm + cur * G1_STAGE;
    const int nxt = cur == G1_NBUF - 1 ? 0 : cur + 1;
    rd(1, b, 1); mm(0);
    rd(0, b, 2); mm(1);
    rd(1, b, 3); mm(0);
    if (t < 14) { WAIT_V(10); } else { WAIT_V(0); }
    BAR();
    if (t < 13) stage(t + 3, cur);
    if (t < 15) rd(0, sm + nxt * G1_STAGE, 0);
    mm(1);
    cur = nxt;
  }
  const int head = 2 * j + wn;
#pragma unroll
  for (int ts = 0; ts < 2; ++ts) {
    char* dst = ws + WS_QF + ((size_t)((rb * 4 + wm * 2 + ts) * 32 + head) * 2) * 1024 + lane * 16;
    *(bf16x8*)dst = pack8(acc[ts][0], 0);
    *(bf16x8*)(dst + 1024) = pack8(acc[ts][0], 1);
  }
  bf16x8 wp[2];
  {
    const char* wb = sm + G1_WPHI + wn * 2048 + (4 * h + q4) * 64 + (2 * blk + (p4 >> 1)) * 16 + (p4 & 1) * 8;
#pragma unroll
    for (int s = 0; s < 2; ++s) wp[s] = cat8(tr_read(wb + s * 1024), tr_read(wb + s * 1024 + 512));
  }
  char* img = sm + wave * 8192;
  f32x16 zero;
#pragma unroll
  for (int i = 0; i < 16; ++i) zero[i] = 0.f;
#pragma unroll
  for (int ts = 0; ts < 2; ++ts) {
    f32x16 pk = mfma32(wp[0], pack8(acc[ts][1], 0), zero);
    pk = mfma32(wp[1], pack8(acc[ts][1], 1), pk);
    bf16x8 pos[2], neg[2];
    dual_softmax(pk, pos, neg);
    const int row = ts * 32 + l31, sw = (row >> 1) & 7;
#pragma unroll
    for (int g = 0; g < 4; ++g) {
      const u32x4 pp = __builtin_bit_cast(u32x4, pos[g >> 1]), nn = __builtin_bit_cast(u32x4, neg[g >> 1]);
      u32x2 a = {pp[(g & 1) * 2], pp[(g & 1) * 2 + 1]}, b = {nn[(g & 1) * 2], nn[(g & 1) * 2 + 1]};
      const int o = row * 64 + (((2 * g + h) ^ sw) << 3);
      *(u32x2*)(img + o) = a;
      *(u32x2*)(img + 4096 + o) = b;
    }
  }
  f32x16 kvacc[2], zacc[2];
  kvacc[0] = zero; kvacc[1] = zero; zacc[0] = zero; zacc[1] = zero;
  bf16x8 ones;
#pragma unroll
  for (int i = 0; i < 8; ++i) ones[i] = (short)0x3f80;
#pragma unroll
  for (int ts = 0; ts < 2; ++ts)
#pragma unroll
    for (int s = 0; s < 2; ++s) {
      const bf16x8 vb = pack8(acc[ts][2], s);
      const int r0 = ts * 32 + 16 * s + 4 * h + q4, r1 = r0 + 8;
      const int o0 = r0 * 64 + (((4 * blk + p4) ^ ((r0 >> 1) & 7)) << 3), o1 = r1 * 64 + (((4 * blk + p4) ^ ((r1 >> 1) & 7)) << 3);
#pragma unroll
      for (int tl = 0; tl < 2; ++tl) {
        const bf16x8 pa = cat8(tr_read(img + tl * 4096 + o0), tr_read(img + tl * 4096 + o1));
        kvacc[tl] = mfma32(pa, vb, kvacc[tl]);
        zacc[tl] = mfma32(pa, ones, zacc[tl]);
      }
    }
  char* red = sm + 32768 + wn * 8704;
  if (wm == 1) {
#pragma unroll
    for (int tl = 0; tl < 2; ++tl)
#pragma unroll
      for (int qd = 0; qd < 4; ++qd) {
        f32x4 t = {kvacc[tl][4 * qd], kvacc[tl][4 * qd + 1], kvacc[tl][4 * qd + 2], kvacc[tl][4 * qd + 3]};
        *(f32x4*)(red + (tl * 4 + qd) * 1024 + lane * 16) = t;
        if (l31 == 0) {
          f32x4 z = {zacc[tl][4 * qd], zacc[tl][4 * qd + 1], zacc[tl][4 * qd + 2], zacc[tl][4 * qd + 3]};
          *(f32x4*)(red + 8192 + ((tl * 2 + h) * 4 + qd) * 16) = z;
        }
      }
  }
  BAR();
  if (wm == 0) {
    char* dst = ws + WS_KVP + (size_t)(rb * 32 + head) * 8192 + lane * 16;
    char* zdst = ws + WS_ZP + (size_t)(rb * 32 + head) * 256;
#pragma unroll
    for (int tl = 0; tl < 2; ++tl)
#pragma unroll
      for (int qd = 0; qd < 4; ++qd) {
        f32x4 t = *(const f32x4*)(red + (tl * 4 + qd) * 1024 + lane * 16);
        t[0] += kvacc[tl][4 * qd]; t[1] += kvacc[tl][4 * qd + 1]; t[2] += kvacc[tl][4 * qd + 2]; t[3] += kvacc[tl][4 * qd + 3];
        *(f32x4*)(dst + (tl * 4 + qd) * 1024) = t;
        if (l31 == 0) {
          f32x4 z = *(const f32x4*)(red + 8192 + ((tl * 2 + h) * 4 + qd) * 16);
          z[0] += zacc[tl][4 * qd]; z[1] += zacc[tl][4 * qd + 1]; z[2] += zacc[tl][4 * qd + 2]; z[3] += zacc[tl][4 * qd + 3];
          *(f32x4*)(zdst + ((tl * 2 + h) * 4 + qd) * 16) = z;
        }
      }
  }
}

__global__ __launch_bounds__(256, 2) void k1_gemm(const u16* x, const u16* W, const u16* wphi, char* ws) {
  extern __shared__ __attribute__((aligned(16))) char sm[];
  role_gemm1(blockIdx.x, sm, x, W, wphi, ws);
}
__global__ __launch_bounds__(256) void k1_a1(const u16* k, const u16* v, const u16* shk, char* ws) {
  __shared__ __attribute__((aligned(16))) char sm[16384];
  role_a1(blockIdx.x, sm, k, v, shk, ws);
}

constexpr int CV_WIN = 16512, CV_XR = 144 * 16, CV_WAVE = CV_WIN + 2 * CV_XR;
constexpr int CV_LDS = 4 * CV_WAVE;

DI int win_addr(int m0) {
  const int u = m0 + 16;
  return ((u >> 3) << 6) + ((((u >> 1) & 3) ^ ((u >> 4) & 3)) << 4) + ((u & 1) << 3);
}

DI void role_conv(int bid, char* sm, const u16* __restrict__ hk, const u16* __restrict__ Dv, char* __restrict__ ws) {
  const int tid = threadIdx.x, lane = tid & 63;
  const int wave = __builtin_amdgcn_readfirstlane(tid >> 6);
  const int ch = bid * 4 + wave;
  char* wl = sm + wave * CV_WAVE;
  char* xl = wl + CV_WIN;
  const u16* hrow = hk + (size_t)ch * 2048;
  const char* xsrc = ws + WS_XT + (size_t)ch * 4096;
  u32x4 hv[4], xv[4]; u32x2 hp[4];
#pragma unroll
  for (int b = 0; b < 4; ++b) {
    const int e0 = b * 512 + lane * 8;
    hv[b] = *(const u32x4*)(hrow + e0);
    hp[b] = *(const u32x2*)(hrow + (e0 > 0 ? e0 - 4 : 0));
    xv[b] = *(const u32x4*)(xsrc + b * 1024 + lane * 16);
  }
  const float dcoef = bf_lo((unsigned)Dv[ch]);
  if (lane == 0) { hp[0][0] = 0u; hp[0][1] = 0u; }
  {
    u32x4 z = {0u, 0u, 0u, 0u};
    if (lane < 8) *(u32x4*)(wl + lane * 16) = z;
    if (lane < 32) *(u32x4*)(xl + (lane >> 4) * CV_XR + (lane & 15) * 16) = z;
  }
#pragma unroll
  for (int b = 0; b < 4; ++b) {
    *(u32x4*)(xl + (b >> 1) * CV_XR + 256 + ((b & 1) * 64 + lane) * 16) = xv[b];
    const unsigned dm2 = hp[b][0], dm1 = hp[b][1], d0 = hv[b][0], d1 = hv[b][1], d2 = hv[b][2], d3 = hv[b][3];
    unsigned R[10];
    R[0] = __builtin_amdgcn_perm(dm2, dm1, 0x07060100u);
    R[1] = __builtin_amdgcn_alignbit(dm1, dm1, 16);
    R[2] = __builtin_amdgcn_perm(dm1, d0, 0x07060100u);
    R[3] = __builtin_amdgcn_alignbit(d0, d0, 16);
    R[4] = __builtin_amdgcn_perm(d0, d1, 0x07060100u);
    R[5] = __builtin_amdgcn_alignbit(d1, d1, 16);
    R[6] = __builtin_amdgcn_perm(d1, d2, 0x07060100u);
    R[7] = __builtin_amdgcn_alignbit(d2, d2, 16);
    R[8] = __builtin_amdgcn_perm(d2, d3, 0x07060100u);
    R[9] = __builtin_amdgcn_alignbit(d3, d3, 16);
    const int i = b * 64 + lane + 2;
    const int sw = (i >> 1) & 3;
#pragma unroll
    for (int r = 0; r < 4; ++r) {
      u32x4 w = {R[2 * r + 2], R[2 * r], R[2 * r + 3], R[2 * r + 1]};
      *(u32x4*)(wl + i * 64 + ((r ^ sw) << 4)) = w;
    }
  }
  const int a = lane & 15, g = lane >> 4;
  const int off = (g & 1) * 16 + (g >> 1) * 8;
  const int cb = 2 * a + 1 - (off >> 3);
  const int xb0 = CV_WIN + (cb & 1) * CV_XR + (((cb >> 1) + 16) << 4);
  f32x4 acc[8];
#pragma unroll
  for (int c = 0; c < 8; ++c) acc[c] = f32x4{0.f, 0.f, 0.f, 0.f};
  bf16x8 A[2][8], B[2][8];
  auto ld = [&](int set, int t0) {
    const int m0 = 32 * t0 + a + off - 8;
    const char* pa0 = wl + win_addr(m0);
    const char* pa1 = wl + win_addr(m0 - 4);
    const char* pb = wl + xb0 - t0 * 32;
#pragma unroll
    for (int i = 0; i < 8; ++i) {
      A[set][i] = cat8(*(const s16x4*)(pa0 + i * 2048), *(const s16x4*)(pa1 + i * 2048));
      B[set][i] = *(const bf16x8*)(pb + i * 256);
    }
  };
  auto mm = [&](int set) {
#pragma unroll
    for (int t1 = 0; t1 < 8; ++t1)
#pragma unroll
      for (int i = 0; i + t1 < 8; ++i) acc[t1 + i] = mfma16(A[set][t1], B[set][i], acc[t1 + i]);
  };
  ld(0, 0);
#pragma unroll 1
  for (int t0 = 0; t0 < 8; t0 += 2) {
    ld(1, t0 + 1); mm(0);
    if (t0 + 2 < 8) ld(0, t0 + 2);
    mm(1);
  }
  char* ydst = ws + WS_YT + (size_t)ch * 4096 + (16 * a + 4 * g) * 2;
  const int xe = CV_WIN + (g >> 1) * CV_XR + ((a + 16) << 4) + (g & 1) * 8;
#pragma unroll
  for (int c = 0; c < 8; ++c) {
    const u32x2 kv = *(const u32x2*)(wl + xe + c * 256);
    const unsigned y01 = pk_bf16(acc[c][0], acc[c][1]), y23 = pk_bf16(acc[c][2], acc[c][3]);
    const unsigned t01 = pk_bf16(bf_lo(kv[0]) * dcoef, bf_hi(kv[0]) * dcoef), t23 = pk_bf16(bf_lo(kv[1]) * dcoef, bf_hi(kv[1]) * dcoef);
    u32x2 o = {pk_bf16(bf_lo(y01) + bf_lo(t01), bf_hi(y01) + bf_hi(t01)), pk_bf16(bf_lo(y23) + bf_lo(t23), bf_hi(y23) + bf_hi(t23))};
    *(u32x2*)(ydst + c * 512) = o;
  }
}

DI void role_o(int bid, char* sm, const u16* __restrict__ wphi, char* __restrict__ ws) {
  const int tid = threadIdx.x, lane = tid & 63, wave = tid >> 6;
  const int head = (bid & 7) * 4 + ((bid >> 3) & 3), tb = bid >> 5;
  const int h = lane >> 5;
  const int blk = (lane >> 4) & 1, q4 = (lane & 15) >> 2, p4 = lane & 3;
  bf16x8 qf[2][2];
#pragma unroll
  for (int ts = 0; ts < 2; ++ts) {
    const char* src = ws + WS_QF + ((size_t)((tb * 8 + wave * 2 + ts) * 32 + head) * 2) * 1024 + lane * 16;
    qf[ts][0] = *(const bf16x8*)src; qf[ts][1] = *(const bf16x8*)(src + 1024);
  }
  if (tid < 128) *(u32x4*)(sm + 8192 + tid * 16) = *(const u32x4*)(wphi + head * 1024 + tid * 8);
  {
    const char* src = ws + WS_KVP + (size_t)head * 8192 + tid * 16;
    f32x4 s0 = {0.f, 0.f, 0.f, 0.f}, s1 = s0, e0[16], e1[16];
#pragma unroll
    for (int r = 0; r < 16; ++r) { e0[r] = *(const f32x4*)(src + (size_t)r * 32 * 8192); e1[r] = *(const f32x4*)(src + (size_t)r * 32 * 8192 + 4096); }
    float zs = 0.f, ze[16];
    if (tid < 64) {
#pragma unroll
      for (int r = 0; r < 16; ++r) ze[r] = *(const float*)(ws + WS_ZP + (size_t)(r * 32 + head) * 256 + tid * 4);
#pragma unroll
      for (int r = 0; r < 16; ++r) zs += ze[r];
    }
#pragma unroll
    for (int r = 0; r < 16; ++r) { s0 += e0[r]; s1 += e1[r]; }
    const int qd = tid >> 6;
    u32x2 a = {pk_bf16(s0[0], s0[1]), pk_bf16(s0[2], s0[3])}, b = {pk_bf16(s1[0], s1[1]), pk_bf16(s1[2], s1[3])};
    *(u32x2*)(sm + (qd >> 1) * 1024 + lane * 16 + (qd & 1) * 8) = a;
    *(u32x2*)(sm + 2048 + (qd >> 1) * 1024 + lane * 16 + (qd & 1) * 8) = b;
    if (tid < 64) *(u16*)(sm + 4096 + tid * 2) = (u16)(pk_bf16(zs, 0.f) & 0xffffu);
  }
  __syncthreads();
  bf16x8 kvf[4], zf[4], wp[2];
#pragma unroll
  for (int i = 0; i < 4; ++i) {
    kvf[i] = *(const bf16x8*)(sm + i * 1024 + lane * 16);
    zf[i] = *(const bf16x8*)(sm + 4096 + ((i >> 1) * 2 + h) * 32 + (i & 1) * 16);
  }
  {
    const char* wb = sm + 8192 + (4 * h + q4) * 64 + (2 * blk + (p4 >> 1)) * 16 + (p4 & 1) * 8;
#pragma unroll
    for (int s = 0; s < 2; ++s) wp[s] = cat8(tr_read(wb + s * 1024), tr_read(wb + s * 1024 + 512));
  }
  f32x16 zero;
#pragma unroll
  for (int i = 0; i < 16; ++i) zero[i] = 0.f;
#pragma unroll
  for (int ts = 0; ts < 2; ++ts) {
    f32x16 pq = mfma32(wp[0], qf[ts][0], zero);
    pq = mfma32(wp[1], qf[ts][1], pq);
    bf16x8 ph[4];
    dual_softmax(pq, ph, ph + 2);
    f32x16 num = zero, den = zero;
#pragma unroll
    for (int i = 0; i < 4; ++i) { num = mfma32(kvf[i], ph[i], num); den = mfma32(zf[i], ph[i], den); }
    const float dn = rbf(rbf(den[0]) + 1e-6f);
    const float rd = 1.0f / dn;
    f32x16 o;
#pragma unroll
    for (int i = 0; i < 8; ++i) {
      const unsigned u = pk_bf16(num[2 * i], num[2 * i + 1]);
      o[2 * i] = bf_lo(u) * rd; o[2 * i + 1] = bf_hi(u) * rd;
    }
    char* dst = ws + WS_OF + ((size_t)((tb * 8 + wave * 2 + ts) * 32 + head) * 2) * 1024 + lane * 16;
    *(bf16x8*)dst = pack8(o, 0);
    *(bf16x8*)(dst + 1024) = pack8(o, 1);
  }
}

__global__ __launch_bounds__(256, 2) void k2_conv(const u16* hk, const u16* Dv, char* ws) {
  extern __shared__ __attribute__((aligned(16))) char sm[];
  role_conv(blockIdx.x, sm, hk, Dv, ws);
}
__global__ __launch_bounds__(256, 2) void k2_o(const u16* wphi, char* ws) {
  __shared__ __attribute__((aligned(16))) char sm[10240];
  role_o(blockIdx.x, sm, wphi, ws);
}

DI void role_a3(int tile, char* sm, const u16* __restrict__ q, const char* __restrict__ ws, u16* __restrict__ out) {
  const int tid = threadIdx.x, cg = tile & 15, rb = tile >> 4;
  const int cgp = tid & 7, rg = tid >> 3;
  const int ch0 = cg * 64 + cgp * 8, r0 = rb * 128 + rg * 4;
  u32x4 qq[4], yy[4];
#pragma unroll
  for (int it = 0; it < 4; ++it) {
    const int ch = it * 16 + (tid >> 4), c = tid & 15;
    yy[it] = *(const u32x4*)(ws + WS_YT + (size_t)(cg * 64 + ch) * 4096 + rb * 256 + c * 16);
  }
#pragma unroll
  for (int i = 0; i < 4; ++i) qq[i] = *(const u32x4*)(q + (size_t)(r0 + i) * D_ + ch0);
#pragma unroll
  for (int it = 0; it < 4; ++it) {
    const int ch = it * 16 + (tid >> 4), c = tid & 15;
    *(u32x4*)(sm + ch * 256 + ((c ^ (ch >> 3)) << 4)) = yy[it];
  }
  __syncthreads();
  u32x2 y[8];
#pragma unroll
  for (int e = 0; e < 8; ++e) y[e] = *(const u32x2*)(sm + (cgp * 8 + e) * 256 + ((rg ^ (2 * cgp)) << 3));
#pragma unroll
  for (int i = 0; i < 4; ++i) {
    u32x4 o;
#pragma unroll
    for (int e2 = 0; e2 < 4; ++e2) {
      const unsigned ya = y[2 * e2][i >> 1], yb = y[2 * e2 + 1][i >> 1];
      const float fa = (i & 1) ? bf_hi(ya) : bf_lo(ya), fb = (i & 1) ? bf_hi(yb) : bf_lo(yb);
      o[e2] = pk_bf16(bf_lo(qq[i][e2]) * fa, bf_hi(qq[i][e2]) * fb);
    }
    *(u32x4*)(out + (size_t)(r0 + i) * 2048 + ch0) = o;
  }
}

constexpr int G2_STAGE = 24576, G2_NBUF = 4, G2_LDS = G2_STAGE * G2_NBUF;

DI void role_gemm2(int bid, char* sm, const u16* __restrict__ wo, const char* __restrict__ ws, u16* __restrict__ out) {
  const int tid = threadIdx.x, lane = tid & 63;
  const int wave = __builtin_amdgcn_readfirstlane(tid >> 6);
  const int xcd = bid & 7, idx = bid >> 3;
  const int rb = (xcd & 1) * 8 + (idx & 7), nb = (xcd >> 1) * 4 + (idx >> 3);
  const int h = lane >> 5, l31 = lane & 31;
  const char* osrc = ws + WS_OF + (size_t)(rb * 4 + wave) * 65536 + lane * 16;
  const u16* wsrc = wo + (size_t)(wave * 16 + ((lane >> 2) & 7)) * 1024 + nb * 64 + (lane >> 5) * 32 + (lane & 3) * 8;
  auto stage = [&](int t, int buf) {
    char* b = sm + buf * G2_STAGE;
#pragma unroll
    for (int i = 0; i < 4; ++i) glds16(osrc + t * 4096 + i * 1024, b + wave * 4096 + i * 1024);
#pragma unroll
    for (int i = 0; i < 2; ++i) glds16(wsrc + (size_t)(t * 64 + i * 8) * 1024, b + 16384 + (wave * 2 + i) * 1024);
  };
  stage(0, 0); stage(1, 1); stage(2, 2); stage(3, 3);
  const int blk = (lane >> 4) & 1, q4 = (lane & 15) >> 2, p4 = lane & 3;
  const int woff = 16384 + (4 * h + q4) * 64 + (2 * blk + (p4 >> 1)) * 16 + (p4 & 1) * 8;
  const int ooff = wave * 4096 + lane * 16;
  f32x16 acc[2];
#pragma unroll
  for (int b = 0; b < 2; ++b)
#pragma unroll
    for (int i = 0; i < 16; ++i) acc[b][i] = 0.f;
  bf16x8 of[2], wf[2][2];
  auto rd = [&](int set, const char* b, int s) {
    of[set] = *(const bf16x8*)(b + ooff + s * 1024);
#pragma unroll
    for (int g = 0; g < 2; ++g) wf[set][g] = cat8(tr_read(b + woff + s * 2048 + g * 512), tr_read(b + woff + s * 2048 + 1024 + g * 512));
  };
  auto mm = [&](int set) {
    acc[0] = mfma32(wf[set][0], of[set], acc[0]);
    acc[1] = mfma32(wf[set][1], of[set], acc[1]);
  };
  WAIT_V(18); BAR();
  rd(0, sm, 0);
  int cur = 0;
#pragma unroll 1
  for (int t = 0; t < 16; ++t) {
    const char* b = sm + cur * G2_STAGE;
    const int nxt = (cur + 1) & 3;
    rd(1, b, 1); mm(0);
    rd(0, b, 2); mm(1);
    rd(1, b, 3); mm(0);
    if (t < 13) { WAIT_V(12); } else if (t == 13) { WAIT_V(6); } else { WAIT_V(0); }
    BAR();
    if (t < 12) stage(t + 4, cur);
    if (t < 15) rd(0, sm + nxt * G2_STAGE, 0);
    mm(1);
    cur = nxt;
  }
  char* ot = sm + wave * 4096;
#pragma unroll
  for (int g = 0; g < 2; ++g)
#pragma unroll
    for (int qd = 0; qd < 4; ++qd) {
      u32x2 w = {pk_bf16(acc[g][4 * qd], acc[g][4 * qd + 1]), pk_bf16(acc[g][4 * qd + 2], acc[g][4 * qd + 3])};
      *(u32x2*)(ot + l31 * 128 + (((4 * g + qd) ^ (l31 & 7)) << 4) + h * 8) = w;
    }
  WAIT_L0();
#pragma unroll
  for (int it = 0; it < 4; ++it) {
    const int row = it * 8 + (lane >> 3), c = lane & 7;
    const u32x4 d = *(const u32x4*)(ot + row * 128 + ((c ^ (row & 7)) << 4));
    *(u32x4*)(out + (size_t)(rb * 128 + wave * 32 + row) * 2048 + 1024 + nb * 64 + c * 8) = d;
  }
}

__global__ __launch_bounds__(256, 2) void k3_gemm(const u16* wo, const char* ws, u16* out) {
  extern __shared__ __attribute__((aligned(16))) char sm[];
  role_gemm2(blockIdx.x, sm, wo, ws, out);
}
__global__ __launch_bounds__(256) void k3_a3(const u16* q, const char* ws, u16* out) {
  __shared__ __attribute__((aligned(16))) char sm[16384];
  role_a3(blockIdx.x, sm, q, ws, out);
}

extern "C" void kernel_launch(void* const* d_in, const int* in_sizes, int n_in, void* d_out, int out_size, void* d_ws, size_t ws_size,
                              hipStream_t stream) {
  const u16 *q = (const u16*)d_in[0], *k = (const u16*)d_in[1], *v = (const u16*)d_in[2], *shk = (const u16*)d_in[3];
  const u16 *hk = (const u16*)d_in[4], *Dv = (const u16*)d_in[5], *x = (const u16*)d_in[6], *W = (const u16*)d_in[7];
  const u16 *wphi = (const u16*)d_in[8], *wo = (const u16*)d_in[9];
  u16* out = (u16*)d_out; char* ws = (char*)d_ws;
  hipFuncSetAttribute((const void*)k1_gemm, hipFuncAttributeMaxDynamicSharedMemorySize, G1_LDS);
  hipFuncSetAttribute((const void*)k2_conv, hipFuncAttributeMaxDynamicSharedMemorySize, CV_LDS);
  hipFuncSetAttribute((const void*)k3_gemm, hipFuncAttributeMaxDynamicSharedMemorySize, G2_LDS);
  k1_a1<<<256, 256, 0, stream>>>(k, v, shk, ws);
  k1_gemm<<<256, 256, G1_LDS, stream>>>(x, W, wphi, ws);
  k2_conv<<<256, 256, CV_LDS, stream>>>(hk, Dv, ws);
  k2_o<<<256, 256, 0, stream>>>(wphi, ws);
  k3_gemm<<<256, 256, G2_LDS, stream>>>(wo, ws, out);
  k3_a3<<<256, 256, 0, stream>>>(q, ws, out);
}
```

```cpp
#include <hip/hip_runtime.h>
#include <stdint.h>
#include <type_traits>

typedef short bf16x8 __attribute__((ext_vector_type(8)));
typedef short s16x4 __attribute__((ext_vector_type(4)));
typedef float f32x4 __attribute__((ext_vector_type(4)));
typedef float f32x2 __attribute__((ext_vector_type(2)));
typedef float f32x16 __attribute__((ext_vector_type(16)));
typedef unsigned u32x4 __attribute__((ext_vector_type(4)));
typedef unsigned u32x2 __attribute__((ext_vector_type(2)));
typedef __bf16 bfx2 __attribute__((ext_vector_type(2)));
typedef unsigned short u16;
#define DI __device__ __forceinline__
#define LAS __attribute__((address_space(3)))

constexpr int S_ = 2048, D_ = 1024;
constexpr size_t WS_XT = 0;
constexpr size_t WS_YT = 4u << 20;
constexpr size_t WS_QF = 8u << 20;
constexpr size_t WS_OF = 12u << 20;
constexpr size_t WS_KVP = 16u << 20;
constexpr size_t WS_ZP = 20u << 20;

DI unsigned pk_bf16(float lo, float hi) { f32x2 p = {lo, hi}; return __builtin_bit_cast(unsigned, __builtin_convertvector(p, bfx2)); }
DI float bf_lo(unsigned u) { return __uint_as_float(u << 16); }
DI float bf_hi(unsigned u) { return __uint_as_float(u & 0xffff0000u); }
DI float rbf(float x) { return bf_lo(pk_bf16(x, 0.f)); }
DI f32x16 mfma32(bf16x8 a, bf16x8 b, f32x16 c) { return __builtin_amdgcn_mfma_f32_32x32x16_bf16(a, b, c, 0, 0, 0); }
DI f32x4 mfma16(bf16x8 a, bf16x8 b, f32x4 c) { return __builtin_amdgcn_mfma_f32_16x16x32_bf16(a, b, c, 0, 0, 0); }
DI s16x4 tr_read(const char* p) { return __builtin_amdgcn_ds_read_tr16_b64_v4i16((LAS s16x4*)p); }
DI bf16x8 cat8(s16x4 a, s16x4 b) { return __builtin_shufflevector(a, b, 0, 1, 2, 3, 4, 5, 6, 7); }
typedef __amdgpu_buffer_rsrc_t rsrc_t;
DI rsrc_t mk_rsrc(const void* p, unsigned bytes) { return __builtin_amdgcn_make_buffer_rsrc((void*)p, 0, bytes, 0x00020000); }
DI void blds16(rsrc_t r, unsigned voff, unsigned soff, void* l) { __builtin_amdgcn_raw_ptr_buffer_load_lds(r, (LAS void*)l, 16, voff, soff, 0, 0); }
DI void glds16(const void* g, void* l) { __builtin_amdgcn_global_load_lds((const unsigned*)g, (unsigned*)l, 16, 0, 0); }
#define WAIT_V(n) asm volatile("s_waitcnt vmcnt(%0)" ::"n"(n) : "memory")
#define WAIT_L0() asm volatile("s_waitcnt lgkmcnt(0)" ::: "memory")
#define BAR() do { WAIT_L0(); __builtin_amdgcn_s_barrier(); asm volatile("" ::: "memory"); } while (0)
#define SCHED0() __builtin_amdgcn_sched_barrier(0)
#define SGB(m, n) __builtin_amdgcn_sched_group_barrier(m, n, 0)
#define G1_SCHED(NV) do { SGB(0x100, 3); SGB(0x8, 1); SGB(0x100, 2); SGB(0x8, 1); SGB(0x100, 3); SGB(0x8, 1); \
    if ((NV) > 0) SGB(0x10, 1); SGB(0x8, 1); if ((NV) > 1) SGB(0x10, 1); SGB(0x8, 1); if ((NV) > 2) SGB(0x10, 1); SGB(0x8, 1); if ((NV) > 3) SGB(0x10, 1); SCHED0(); } while (0)
#define ST(kid, i)
DI float xhalf(float v) { return __shfl_xor(v, 32); }
DI bf16x8 pack8(const f32x16& x, int s) {
  u32x4 p;
  p[0] = pk_bf16(x[8 * s + 0], x[8 * s + 1]); p[1] = pk_bf16(x[8 * s + 2], x[8 * s + 3]);
  p[2] = pk_bf16(x[8 * s + 4], x[8 * s + 5]); p[3] = pk_bf16(x[8 * s + 6], x[8 * s + 7]);
  return __builtin_bit_cast(bf16x8, p);
}
DI void dual_softmax(const f32x16& pin, bf16x8* pos, bf16x8* neg) {
  float p[16];
#pragma unroll
  for (int i = 0; i < 8; ++i) { unsigned u = pk_bf16(pin[2 * i], pin[2 * i + 1]); p[2 * i] = bf_lo(u); p[2 * i + 1] = bf_hi(u); }
  float mx = p[0], mn = p[0];
#pragma unroll
  for (int i = 1; i < 16; ++i) { mx = fmaxf(mx, p[i]); mn = fminf(mn, p[i]); }
  mx = fmaxf(mx, xhalf(mx)); mn = fminf(mn, xhalf(mn));
  const float L2E = 1.4426950408889634f;
  float ep[16], en[16], sp = 0.f, sn = 0.f;
  const float mxl = mx * L2E, mnl = mn * L2E;
#pragma unroll
  for (int i = 0; i < 16; ++i) {
    ep[i] = __builtin_amdgcn_exp2f(fmaf(p[i], L2E, -mxl)); en[i] = __builtin_amdgcn_exp2f(fmaf(p[i], -L2E, mnl));
    sp += ep[i]; sn += en[i];
  }
  sp += xhalf(sp); sn += xhalf(sn);
  const float rp = __builtin_amdgcn_rcpf(sp), rn = __builtin_amdgcn_rcpf(sn);
#pragma unroll
  for (int s = 0; s < 2; ++s) {
    u32x4 a, b;
#pragma unroll
    for (int i = 0; i < 4; ++i) {
      a[i] = pk_bf16(ep[8 * s + 2 * i] * rp, ep[8 * s + 2 * i + 1] * rp);
      b[i] = pk_bf16(en[8 * s + 2 * i] * rn, en[8 * s + 2 * i + 1] * rn);
    }
    pos[s] = __builtin_bit_cast(bf16x8, a); neg[s] = __builtin_bit_cast(bf16x8, b);
  }
}

DI void role_a1(int tile, char* sm, const u16* __restrict__ k, const u16* __restrict__ v, const u16* __restrict__ shk, char* __restrict__ ws) {
  const int tid = threadIdx.x, cg = tile & 15, rb = tile >> 4;
  const int cgp = tid & 7, rg = tid >> 3;
  const int ch0 = cg * 64 + cgp * 8, r0 = rb * 128 + rg * 4;
  u32x4 kk[7], vv[4], sk[4];
#pragma unroll
  for (int i = 0; i < 7; ++i) {
    int r = r0 - 3 + i;
    kk[i] = *(const u32x4*)(k + (size_t)(r < 0 ? 0 : r) * D_ + ch0);
    if (r < 0) kk[i] = u32x4{0u, 0u, 0u, 0u};
  }
#pragma unroll
  for (int i = 0; i < 4; ++i) vv[i] = *(const u32x4*)(v + (size_t)(r0 + i) * D_ + ch0);
#pragma unroll
  for (int i = 0; i < 4; ++i) sk[i] = *(const u32x4*)(shk + ch0 * 4 + i * 8);
#pragma unroll
  for (int e = 0; e < 8; ++e) {
    const unsigned w01 = sk[e >> 1][(e & 1) * 2], w23 = sk[e >> 1][(e & 1) * 2 + 1];
    const float w[4] = {bf_lo(w01), bf_hi(w01), bf_lo(w23), bf_hi(w23)};
    float kf[7];
#pragma unroll
    for (int i = 0; i < 7; ++i) kf[i] = (e & 1) ? bf_hi(kk[i][e >> 1]) : bf_lo(kk[i][e >> 1]);
    float o[4];
#pragma unroll
    for (int i = 0; i < 4; ++i) {
      float a = rbf(kf[i] * w[3]);
      a = rbf(a + rbf(kf[i + 1] * w[2]));
      a = rbf(a + rbf(kf[i + 2] * w[1]));
      a = rbf(a + rbf(kf[i + 3] * w[0]));
      const float vf = (e & 1) ? bf_hi(vv[i][e >> 1]) : bf_lo(vv[i][e >> 1]);
      o[i] = a * vf;
    }
    u32x2 w2 = {pk_bf16(o[0], o[1]), pk_bf16(o[2], o[3])};
    *(u32x2*)(sm + (cgp * 8 + e) * 256 + ((rg ^ (2 * cgp)) << 3)) = w2;
  }
  __syncthreads();
#pragma unroll
  for (int it = 0; it < 4; ++it) {
    const int ch = it * 16 + (tid >> 4), l = tid & 15;
    const int c = ((l & 7) << 1) | (l >> 3);
    u32x4 d = *(const u32x4*)(sm + ch * 256 + ((c ^ (ch >> 3)) << 4));
    const int gc = rb * 16 + c;
    *(u32x4*)(ws + WS_XT + (size_t)(cg * 64 + ch) * 4096 + (gc & 1) * 2048 + (gc >> 1) * 16) = d;
  }
}

constexpr int G1_STAGE = 40960, G1_NBUF = 3, G1_WPHI = G1_STAGE * G1_NBUF;
constexpr int G1_LDS = G1_WPHI + 4096;

DI void role_gemm1(int bid, char* sm, const u16* __restrict__ x, const u16* __restrict__ W, const u16* __restrict__ wphi, char* __restrict__ ws) {
  const int tid = threadIdx.x, lane = tid & 63;
  const int wave = __builtin_amdgcn_readfirstlane(tid >> 6);
  const int xcd = bid & 7, idx = bid >> 3;
  const int rb = (xcd & 1) * 8 + (idx & 7), j = (xcd >> 1) * 4 + (idx >> 3);
  const int wm = wave & 1, wn = wave >> 1;
  const int h = lane >> 5, l31 = lane & 31;
  ST(0,0);
  const rsrc_t rx = mk_rsrc(x, 2048 * 1024 * 2), rw = mk_rsrc(W, 1024 * 3072 * 2), rp = mk_rsrc(wphi, 65536);
  unsigned xs[4], wsrc[6];
#pragma unroll
  for (int i = 0; i < 4; ++i) {
    const int p = wave * 4 + i, row = p * 8 + (lane >> 3), ch = (lane & 7) ^ ((row >> 1) & 7);
    xs[i] = ((rb * 128 + row) * 1024 + ch * 8) * 2;
  }
#pragma unroll
  for (int i = 0; i < 6; ++i) {
    const int p = wave * 6 + i, kg = p / 3, seg = p % 3;
    wsrc[i] = ((kg * 8 + ((lane >> 2) & 7)) * 3072 + seg * 1024 + j * 64 + (lane >> 5) * 32 + (lane & 3) * 8) * 2;
  }
  auto piece = [&](int t, int buf, int i) {
    char* b = sm + buf * G1_STAGE;
    if (i < 4) blds16(rx, xs[i], t * 128, b + (wave * 4 + i) * 1024);
    else blds16(rw, wsrc[i - 4], t * (64 * 3072 * 2), b + 16384 + (wave * 6 + i - 4) * 1024);
  };
  blds16(rp, (j * 2048 + wave * 512 + lane * 8) * 2, 0, sm + G1_WPHI + wave * 1024);
#pragma unroll
  for (int i = 0; i < 10; ++i) piece(0, 0, i);
#pragma unroll
  for (int i = 0; i < 10; ++i) piece(1, 1, i);
#pragma unroll
  for (int i = 0; i < 4; ++i) piece(2, 2, i);
  int xoff[4];
  {
    const int row = wm * 64 + l31, sw = (row >> 1) & 7;
#pragma unroll
    for (int s = 0; s < 4; ++s) xoff[s] = row * 128 + (((2 * s + h) ^ sw) << 4);
  }
  const int blk = (lane >> 4) & 1, q4 = (lane & 15) >> 2, p4 = lane & 3;
  const int woff = 16384 + h * 3072 + wn * 512 + q4 * 64 + (2 * blk + (p4 >> 1)) * 16 + (p4 & 1) * 8;
  f32x16 acc[2][3];
#pragma unroll
  for (int a = 0; a < 2; ++a)
#pragma unroll
    for (int b = 0; b < 3; ++b)
#pragma unroll
      for (int i = 0; i < 16; ++i) acc[a][b][i] = 0.f;
  bf16x8 xf[2][2], wf[2][3];
  auto rd = [&](int set, const char* b, int s) {
    xf[set][0] = *(const bf16x8*)(b + xoff[s]);
#pragma unroll
    for (int g = 0; g < 3; ++g) wf[set][g] = cat8(tr_read(b + woff + s * 6144 + g * 1024), tr_read(b + woff + s * 6144 + g * 1024 + 256));
    xf[set][1] = *(const bf16x8*)(b + xoff[s] + 4096);
  };
  auto mm = [&](int set) {
#pragma unroll
    for (int ts = 0; ts < 2; ++ts) {
      acc[ts][0] = mfma32(wf[set][0], xf[set][ts], acc[ts][0]);
      acc[ts][1] = mfma32(wf[set][1], xf[set][ts], acc[ts][1]);
      acc[ts][2] = mfma32(xf[set][ts], wf[set][2], acc[ts][2]);
    }
  };
  ST(0,1);
  WAIT_V(14); BAR();
  ST(0,2);
  rd(0, sm, 0);
  SCHED0();
  int cur = 0, nxt = 1, nx2 = 2;
  auto step = [&](int t, auto BC, auto AA, auto WN, auto RN) {
    const char* b = sm + cur * G1_STAGE;
    rd(1, b, 1);
    if constexpr (BC.value) { piece(t + 2, nx2, 4); piece(t + 2, nx2, 5); piece(t + 2, nx2, 6); }
    mm(0);
    G1_SCHED(BC.value ? 3 : 0);
    rd(0, b, 2);
    if constexpr (BC.value) { piece(t + 2, nx2, 7); piece(t + 2, nx2, 8); piece(t + 2, nx2, 9); }
    mm(1);
    G1_SCHED(BC.value ? 3 : 0);
    rd(1, b, 3); mm(0);
    G1_SCHED(0);
    WAIT_V(WN.value);
    BAR();
    SCHED0();
    if constexpr (RN.value) rd(0, sm + nxt * G1_STAGE, 0);
    if constexpr (AA.value) { piece(t + 3, cur, 0); piece(t + 3, cur, 1); piece(t + 3, cur, 2); piece(t + 3, cur, 3); }
    mm(1);
    G1_SCHED(AA.value ? 4 : 0);
    const int o = cur; cur = nxt; nxt = nx2; nx2 = o;
  };
  using T_ = std::true_type; using F_ = std::false_type;
#pragma unroll 1
  for (int t = 0; t < 13; ++t) step(t, T_{}, T_{}, std::integral_constant<int, 10>{}, T_{});
  step(13, T_{}, F_{}, std::integral_constant<int, 10>{}, T_{});
  step(14, F_{}, F_{}, std::integral_constant<int, 0>{}, T_{});
  step(15, F_{}, F_{}, std::integral_constant<int, 0>{}, F_{});
  ST(0,3);
  const int head = 2 * j + wn;
#pragma unroll
  for (int ts = 0; ts < 2; ++ts) {
    char* dst = ws + WS_QF + ((size_t)((rb * 4 + wm * 2 + ts) * 32 + head) * 2) * 1024 + lane * 16;
    *(bf16x8*)dst = pack8(acc[ts][0], 0);
    *(bf16x8*)(dst + 1024) = pack8(acc[ts][0], 1);
  }
  bf16x8 wp[2];
  {
    const char* wb = sm + G1_WPHI + wn * 2048 + (4 * h + q4) * 64 + (2 * blk + (p4 >> 1)) * 16 + (p4 & 1) * 8;
#pragma unroll
    for (int s = 0; s < 2; ++s) wp[s] = cat8(tr_read(wb + s * 1024), tr_read(wb + s * 1024 + 512));
  }
  char* img = sm + wave * 8192;
  f32x16 zero;
#pragma unroll
  for (int i = 0; i < 16; ++i) zero[i] = 0.f;
#pragma unroll
  for (int ts = 0; ts < 2; ++ts) {
    f32x16 pk = mfma32(wp[0], pack8(acc[ts][1], 0), zero);
    pk = mfma32(wp[1], pack8(acc[ts][1], 1), pk);
    bf16x8 pos[2], neg[2];
    dual_softmax(pk, pos, neg);
    const int row = ts * 32 + l31, sw = (row >> 1) & 7;
#pragma unroll
    for (int g = 0; g < 4; ++g) {
      const u32x4 pp = __builtin_bit_cast(u32x4, pos[g >> 1]), nn = __builtin_bit_cast(u32x4, neg[g >> 1]);
      u32x2 a = {pp[(g & 1) * 2], pp[(g & 1) * 2 + 1]}, b = {nn[(g & 1) * 2], nn[(g & 1) * 2 + 1]};
      const int o = row * 64 + (((2 * g + h) ^ sw) << 3);
      *(u32x2*)(img + o) = a;
      *(u32x2*)(img + 4096 + o) = b;
    }
  }
  ST(0,4);
  f32x16 kvacc[2], zacc[2];
  kvacc[0] = zero; kvacc[1] = zero; zacc[0] = zero; zacc[1] = zero;
  bf16x8 ones;
#pragma unroll
  for (int i = 0; i < 8; ++i) ones[i] = (short)0x3f80;
#pragma unroll
  for (int ts = 0; ts < 2; ++ts)
#pragma unroll
    for (int s = 0; s < 2; ++s) {
      const bf16x8 vb = pack8(acc[ts][2], s);
      const int r0 = ts * 32 + 16 * s + 4 * h + q4, r1 = r0 + 8;
      const int o0 = r0 * 64 + (((4 * blk + p4) ^ ((r0 >> 1) & 7)) << 3), o1 = r1 * 64 + (((4 * blk + p4) ^ ((r1 >> 1) & 7)) << 3);
#pragma unroll
      for (int tl = 0; tl < 2; ++tl) {
        const bf16x8 pa = cat8(tr_read(img + tl * 4096 + o0), tr_read(img + tl * 4096 + o1));
        kvacc[tl] = mfma32(pa, vb, kvacc[tl]);
        zacc[tl] = mfma32(pa, ones, zacc[tl]);
      }
    }
  ST(0,5);
  char* red = sm + 32768 + wn * 8704;
  if (wm == 1) {
#pragma unroll
    for (int tl = 0; tl < 2; ++tl)
#pragma unroll
      for (int qd = 0; qd < 4; ++qd) {
        f32x4 t = {kvacc[tl][4 * qd], kvacc[tl][4 * qd + 1], kvacc[tl][4 * qd + 2], kvacc[tl][4 * qd + 3]};
        *(f32x4*)(red + (tl * 4 + qd) * 1024 + lane * 16) = t;
        if (l31 == 0) {
          f32x4 z = {zacc[tl][4 * qd], zacc[tl][4 * qd + 1], zacc[tl][4 * qd + 2], zacc[tl][4 * qd + 3]};
          *(f32x4*)(red + 8192 + ((tl * 2 + h) * 4 + qd) * 16) = z;
        }
      }
  }
  BAR();
  if (wm == 0) {
    char* dst = ws + WS_KVP + (size_t)(rb * 32 + head) * 8192 + lane * 16;
    char* zdst = ws + WS_ZP + (size_t)(rb * 32 + head) * 256;
#pragma unroll
    for (int tl = 0; tl < 2; ++tl)
#pragma unroll
      for (int qd = 0; qd < 4; ++qd) {
        f32x4 t = *(const f32x4*)(red + (tl * 4 + qd) * 1024 + lane * 16);
        t[0] += kvacc[tl][4 * qd]; t[1] += kvacc[tl][4 * qd + 1]; t[2] += kvacc[tl][4 * qd + 2]; t[3] += kvacc[tl][4 * qd + 3];
        *(f32x4*)(dst + (tl * 4 + qd) * 1024) = t;
        if (l31 == 0) {
          f32x4 z = *(const f32x4*)(red + 8192 + ((tl * 2 + h) * 4 + qd) * 16);
          z[0] += zacc[tl][4 * qd]; z[1] += zacc[tl][4 * qd + 1]; z[2] += zacc[tl][4 * qd + 2]; z[3] += zacc[tl][4 * qd + 3];
          *(f32x4*)(zdst + ((tl * 2 + h) * 4 + qd) * 16) = z;
        }
      }
  }
  ST(0,6);
}

__global__ __launch_bounds__(256, 2) void k1_gemm(const u16* x, const u16* W, const u16* wphi, char* ws) {
  extern __shared__ __attribute__((aligned(16))) char sm[];
  role_gemm1(blockIdx.x, sm, x, W, wphi, ws);
}
__global__ __launch_bounds__(256) void k1_a1(const u16* k, const u16* v, const u16* shk, char* ws) {
  __shared__ __attribute__((aligned(16))) char sm[16384];
  role_a1(blockIdx.x, sm, k, v, shk, ws);
}

constexpr int CV_WIN = 16512 + 512 + 64, CV_XR = 144 * 16, CV_WAVE = CV_WIN + 2 * CV_XR;
constexpr int CV_LDS = 4 * CV_WAVE;

DI int win_addr(int m0) {
  const int u = m0 + 16;
  return ((u >> 3) << 6) + ((u >> 8) << 6) + ((((u >> 1) & 3) ^ ((u >> 4) & 3)) << 4) + ((u & 1) << 3);
}

DI void role_conv(int bid, char* sm, const u16* __restrict__ hk, const u16* __restrict__ Dv, char* __restrict__ ws) {
  const int tid = threadIdx.x, lane = tid & 63;
  const int wave = __builtin_amdgcn_readfirstlane(tid >> 6);
  const int ch = bid * 4 + wave;
  char* wl = sm + wave * CV_WAVE;
  char* xl = wl + CV_WIN;
  const u16* hrow = hk + (size_t)ch * 2048;
  const char* xsrc = ws + WS_XT + (size_t)ch * 4096;
  ST(1,0);
  u32x4 hv[4], xv[4]; u32x2 hp[4];
#pragma unroll
  for (int b = 0; b < 4; ++b) {
    const int e0 = b * 512 + lane * 8;
    hv[b] = *(const u32x4*)(hrow + e0);
    hp[b] = *(const u32x2*)(hrow + (e0 > 0 ? e0 - 4 : 0));
    xv[b] = *(const u32x4*)(xsrc + b * 1024 + lane * 16);
  }
  const float dcoef = bf_lo((unsigned)Dv[ch]);
  if (lane == 0) { hp[0][0] = 0u; hp[0][1] = 0u; }
  {
    u32x4 z = {0u, 0u, 0u, 0u};
    if (lane < 8) *(u32x4*)(wl + lane * 16) = z;
    if (lane < 32) *(u32x4*)(xl + (lane >> 4) * CV_XR + (lane & 15) * 16) = z;
  }
#pragma unroll
  for (int b = 0; b < 4; ++b) {
    *(u32x4*)(xl + (b >> 1) * CV_XR + 256 + ((b & 1) * 64 + lane) * 16) = xv[b];
    const unsigned dm2 = hp[b][0], dm1 = hp[b][1], d0 = hv[b][0], d1 = hv[b][1], d2 = hv[b][2], d3 = hv[b][3];
    unsigned R[10];
    R[0] = __builtin_amdgcn_perm(dm2, dm1, 0x07060100u);
    R[1] = __builtin_amdgcn_alignbit(dm1, dm1, 16);
    R[2] = __builtin_amdgcn_perm(dm1, d0, 0x07060100u);
    R[3] = __builtin_amdgcn_alignbit(d0, d0, 16);
    R[4] = __builtin_amdgcn_perm(d0, d1, 0x07060100u);
    R[5] = __builtin_amdgcn_alignbit(d1, d1, 16);
    R[6] = __builtin_amdgcn_perm(d1, d2, 0x07060100u);
    R[7] = __builtin_amdgcn_alignbit(d2, d2, 16);
    R[8] = __builtin_amdgcn_perm(d2, d3, 0x07060100u);
    R[9] = __builtin_amdgcn_alignbit(d3, d3, 16);
    const int i = b * 64 + lane + 2;
    const int sw = (i >> 1) & 3;
#pragma unroll
    for (int r = 0; r < 4; ++r) {
      u32x4 w = {R[2 * r + 2], R[2 * r], R[2 * r + 3], R[2 * r + 1]};
      *(u32x4*)(wl + i * 64 + ((i >> 5) << 6) + ((r ^ sw) << 4)) = w;
    }
  }
  ST(1,1);
  const int a = lane & 15, g = lane >> 4;
  const int off = (g & 1) * 16 + (g >> 1) * 8;
  const int cb = 2 * a + 1 - (off >> 3);
  const int xb0 = CV_WIN + (cb & 1) * CV_XR + (((cb >> 1) + 16) << 4);
  f32x4 acc[8];
#pragma unroll
  for (int c = 0; c < 8; ++c) acc[c] = f32x4{0.f, 0.f, 0.f, 0.f};
  bf16x8 A[2][8], B[2][8];
  auto ld = [&](int set, int t0) {
    const int m0 = 32 * t0 + a + off - 8;
    const char* pa0 = wl + win_addr(m0);
    const char* pa1 = wl + win_addr(m0 - 4);
    const char* pb = wl + xb0 - t0 * 32;
#pragma unroll
    for (int i = 0; i < 8; ++i) {
      A[set][i] = cat8(*(const s16x4*)(pa0 + i * 2112), *(const s16x4*)(pa1 + i * 2112));
      B[set][i] = *(const bf16x8*)(pb + i * 256);
    }
  };
  auto mm = [&](int set) {
#pragma unroll
    for (int t1 = 0; t1 < 8; ++t1)
#pragma unroll
      for (int i = 0; i + t1 < 8; ++i) acc[t1 + i] = mfma16(A[set][t1], B[set][i], acc[t1 + i]);
  };
  ld(0, 0);
  SCHED0();
#define CV_SCHED() do { _Pragma("unroll") for (int z = 0; z < 24; ++z) { SGB(0x8, 1); SGB(0x100, 1); } SGB(0x8, 12); SCHED0(); } while (0)
#pragma unroll 1
  for (int t0 = 0; t0 < 6; t0 += 2) {
    ld(1, t0 + 1); mm(0); CV_SCHED();
    ld(0, t0 + 2); mm(1); CV_SCHED();
  }
  ld(1, 7); mm(0); CV_SCHED();
  mm(1);
  ST(1,2);
  char* ydst = ws + WS_YT + (size_t)ch * 4096 + (16 * a + 4 * g) * 2;
  const int xe = CV_WIN + (g >> 1) * CV_XR + ((a + 16) << 4) + (g & 1) * 8;
#pragma unroll
  for (int c = 0; c < 8; ++c) {
    const u32x2 kv = *(const u32x2*)(wl + xe + c * 256);
    const unsigned y01 = pk_bf16(acc[c][0], acc[c][1]), y23 = pk_bf16(acc[c][2], acc[c][3]);
    const unsigned t01 = pk_bf16(bf_lo(kv[0]) * dcoef, bf_hi(kv[0]) * dcoef), t23 = pk_bf16(bf_lo(kv[1]) * dcoef, bf_hi(kv[1]) * dcoef);
    u32x2 o = {pk_bf16(bf_lo(y01) + bf_lo(t01), bf_hi(y01) + bf_hi(t01)), pk_bf16(bf_lo(y23) + bf_lo(t23), bf_hi(y23) + bf_hi(t23))};
    *(u32x2*)(ydst + c * 512) = o;
  }
  ST(1,3);
}

DI void role_o(int bid, char* sm, const u16* __restrict__ wphi, char* __restrict__ ws) {
  const int tid = threadIdx.x, lane = tid & 63, wave = tid >> 6;
  const int head = (bid & 7) * 4 + ((bid >> 3) & 3), tb = bid >> 5;
  const int h = lane >> 5;
  const int blk = (lane >> 4) & 1, q4 = (lane & 15) >> 2, p4 = lane & 3;
  bf16x8 qf[2][2];
#pragma unroll
  for (int ts = 0; ts < 2; ++ts) {
    const char* src = ws + WS_QF + ((size_t)((tb * 8 + wave * 2 + ts) * 32 + head) * 2) * 1024 + lane * 16;
    qf[ts][0] = *(const bf16x8*)src; qf[ts][1] = *(const bf16x8*)(src + 1024);
  }
  if (tid < 128) *(u32x4*)(sm + 8192 + tid * 16) = *(const u32x4*)(wphi + head * 1024 + tid * 8);
  {
    const char* src = ws + WS_KVP + (size_t)head * 8192 + tid * 16;
    f32x4 s0 = {0.f, 0.f, 0.f, 0.f}, s1 = s0, e0[16], e1[16];
#pragma unroll
    for (int r = 0; r < 16; ++r) { e0[r] = *(const f32x4*)(src + (size_t)r * 32 * 8192); e1[r] = *(const f32x4*)(src + (size_t)r * 32 * 8192 + 4096); }
    float zs = 0.f, ze[16];
    if (tid < 64) {
#pragma unroll
      for (int r = 0; r < 16; ++r) ze[r] = *(const float*)(ws + WS_ZP + (size_t)(r * 32 + head) * 256 + tid * 4);
#pragma unroll
      for (int r = 0; r < 16; ++r) zs += ze[r];
    }
#pragma unroll
    for (int r = 0; r < 16; ++r) { s0 += e0[r]; s1 += e1[r]; }
    const int qd = tid >> 6;
    u32x2 a = {pk_bf16(s0[0], s0[1]), pk_bf16(s0[2], s0[3])}, b = {pk_bf16(s1[0], s1[1]), pk_bf16(s1[2], s1[3])};
    *(u32x2*)(sm + (qd >> 1) * 1024 + lane * 16 + (qd & 1) * 8) = a;
    *(u32x2*)(sm + 2048 + (qd >> 1) * 1024 + lane * 16 + (qd & 1) * 8) = b;
    if (tid < 64) *(u16*)(sm + 4096 + tid * 2) = (u16)(pk_bf16(zs, 0.f) & 0xffffu);
  }
  __syncthreads();
  bf16x8 kvf[4], zf[4], wp[2];
#pragma unroll
  for (int i = 0; i < 4; ++i) {
    kvf[i] = *(const bf16x8*)(sm + i * 1024 + lane * 16);
    zf[i] = *(const bf16x8*)(sm + 4096 + ((i >> 1) * 2 + h) * 32 + (i & 1) * 16);
  }
  {
    const char* wb = sm + 8192 + (4 * h + q4) * 64 + (2 * blk + (p4 >> 1)) * 16 + (p4 & 1) * 8;
#pragma unroll
    for (int s = 0; s < 2; ++s) wp[s] = cat8(tr_read(wb + s * 1024), tr_read(wb + s * 1024 + 512));
  }
  f32x16 zero;
#pragma unroll
  for (int i = 0; i < 16; ++i) zero[i] = 0.f;
#pragma unroll
  for (int ts = 0; ts < 2; ++ts) {
    f32x16 pq = mfma32(wp[0], qf[ts][0], zero);
    pq = mfma32(wp[1], qf[ts][1], pq);
    bf16x8 ph[4];
    dual_softmax(pq, ph, ph + 2);
    f32x16 num = zero, den = zero;
#pragma unroll
    for (int i = 0; i < 4; ++i) { num = mfma32(kvf[i], ph[i], num); den = mfma32(zf[i], ph[i], den); }
    const float dn = rbf(rbf(den[0]) + 1e-6f);
    const float rd = 1.0f / dn;
    f32x16 o;
#pragma unroll
    for (int i = 0; i < 8; ++i) {
      const unsigned u = pk_bf16(num[2 * i], num[2 * i + 1]);
      o[2 * i] = bf_lo(u) * rd; o[2 * i + 1] = bf_hi(u) * rd;
    }
    char* dst = ws + WS_OF + ((size_t)((tb * 8 + wave * 2 + ts) * 32 + head) * 2) * 1024 + lane * 16;
    *(bf16x8*)dst = pack8(o, 0);
    *(bf16x8*)(dst + 1024) = pack8(o, 1);
  }
}

__global__ __launch_bounds__(256, 2) void k2_conv(const u16* hk, const u16* Dv, char* ws) {
  extern __shared__ __attribute__((aligned(16))) char sm[];
  role_conv(blockIdx.x, sm, hk, Dv, ws);
}
__global__ __launch_bounds__(256, 2) void k2_o(const u16* wphi, char* ws) {
  __shared__ __attribute__((aligned(16))) char sm[10240];
  role_o(blockIdx.x, sm, wphi, ws);
}

DI void role_a3(int tile, char* sm, const u16* __restrict__ q, const char* __restrict__ ws, u16* __restrict__ out) {
  const int tid = threadIdx.x, cg = tile & 15, rb = tile >> 4;
  const int cgp = tid & 7, rg = tid >> 3;
  const int ch0 = cg * 64 + cgp * 8, r0 = rb * 128 + rg * 4;
  u32x4 qq[4], yy[4];
#pragma unroll
  for (int it = 0; it < 4; ++it) {
    const int ch = it * 16 + (tid >> 4), c = tid & 15;
    yy[it] = *(const u32x4*)(ws + WS_YT + (size_t)(cg * 64 + ch) * 4096 + rb * 256 + c * 16);
  }
#pragma unroll
  for (int i = 0; i < 4; ++i) qq[i] = *(const u32x4*)(q + (size_t)(r0 + i) * D_ + ch0);
#pragma unroll
  for (int it = 0; it < 4; ++it) {
    const int ch = it * 16 + (tid >> 4), c = tid & 15;
    *(u32x4*)(sm + ch * 256 + ((c ^ (ch >> 3)) << 4)) = yy[it];
  }
  __syncthreads();
  u32x2 y[8];
#pragma unroll
  for (int e = 0; e < 8; ++e) y[e] = *(const u32x2*)(sm + (cgp * 8 + e) * 256 + ((rg ^ (2 * cgp)) << 3));
#pragma unroll
  for (int i = 0; i < 4; ++i) {
    u32x4 o;
#pragma unroll
    for (int e2 = 0; e2 < 4; ++e2) {
      const unsigned ya = y[2 * e2][i >> 1], yb = y[2 * e2 + 1][i >> 1];
      const float fa = (i & 1) ? bf_hi(ya) : bf_lo(ya), fb = (i & 1) ? bf_hi(yb) : bf_lo(yb);
      o[e2] = pk_bf16(bf_lo(qq[i][e2]) * fa, bf_hi(qq[i][e2]) * fb);
    }
    *(u32x4*)(out + (size_t)(r0 + i) * 2048 + ch0) = o;
  }
}

constexpr int G2_STAGE = 24576, G2_NBUF = 4, G2_LDS = G2_STAGE * G2_NBUF;

DI void role_gemm2(int bid, char* sm, const u16* __restrict__ wo, const char* __restrict__ ws, u16* __restrict__ out) {
  const int tid = threadIdx.x, lane = tid & 63;
  const int wave = __builtin_amdgcn_readfirstlane(tid >> 6);
  const int xcd = bid & 7, idx = bid >> 3;
  const int rb = (xcd & 1) * 8 + (idx & 7), nb = (xcd >> 1) * 4 + (idx >> 3);
  const int h = lane >> 5, l31 = lane & 31;
  ST(2,0);
  const rsrc_t ro = mk_rsrc(ws + WS_OF, 4u << 20), rw = mk_rsrc(wo, 2u << 20);
  const unsigned osrc = (rb * 4 + wave) * 65536 + lane * 16;
  const unsigned wsrc = ((wave * 16 + ((lane >> 2) & 7)) * 1024 + nb * 64 + (lane >> 5) * 32 + (lane & 3) * 8) * 2;
  auto piece = [&](int t, int buf, int i) {
    char* b = sm + buf * G2_STAGE;
    if (i < 4) blds16(ro, osrc + i * 1024, t * 4096, b + wave * 4096 + i * 1024);
    else blds16(rw, wsrc + (i - 4) * 16384, t * 131072, b + 16384 + (wave * 2 + i - 4) * 1024);
  };
#pragma unroll
  for (int t = 0; t < 3; ++t)
#pragma unroll
    for (int i = 0; i < 6; ++i) piece(t, t, i);
  piece(3, 3, 0); piece(3, 3, 1);
  const int blk = (lane >> 4) & 1, q4 = (lane & 15) >> 2, p4 = lane & 3;
  const int woff = 16384 + (4 * h + q4) * 64 + (2 * blk + (p4 >> 1)) * 16 + (p4 & 1) * 8;
  const int ooff = wave * 4096 + lane * 16;
  f32x16 acc[2];
#pragma unroll
  for (int b = 0; b < 2; ++b)
#pragma unroll
    for (int i = 0; i < 16; ++i) acc[b][i] = 0.f;
  bf16x8 of[2], wf[2][2];
  auto rd = [&](int set, const char* b, int s) {
    of[set] = *(const bf16x8*)(b + ooff + s * 1024);
#pragma unroll
    for (int g = 0; g < 2; ++g) wf[set][g] = cat8(tr_read(b + woff + s * 2048 + g * 512), tr_read(b + woff + s * 2048 + 1024 + g * 512));
  };
  auto mm = [&](int set) {
    acc[0] = mfma32(wf[set][0], of[set], acc[0]);
    acc[1] = mfma32(wf[set][1], of[set], acc[1]);
  };
  ST(2,1);
  WAIT_V(14); BAR();
  ST(2,2);
  rd(0, sm, 0);
  SCHED0();
  int cur = 0;
#define G2_SCHED(NV) do { SGB(0x100, 3); SGB(0x8, 1); SGB(0x100, 2); if ((NV) > 0) SGB(0x10, 1); SGB(0x8, 1); if ((NV) > 1) SGB(0x10, 1); SCHED0(); } while (0)
  auto step = [&](int t, auto BC, auto AA, auto WN, auto RN) {
    const char* b = sm + cur * G2_STAGE;
    const int nxt = (cur + 1) & 3, prv = (cur + 3) & 3;
    rd(1, b, 1);
    if constexpr (BC.value) { piece(t + 3, prv, 2); piece(t + 3, prv, 3); }
    mm(0); G2_SCHED(BC.value ? 2 : 0);
    rd(0, b, 2);
    if constexpr (BC.value) { piece(t + 3, prv, 4); piece(t + 3, prv, 5); }
    mm(1); G2_SCHED(BC.value ? 2 : 0);
    rd(1, b, 3); mm(0); G2_SCHED(0);
    WAIT_V(WN.value);
    BAR();
    SCHED0();
    if constexpr (RN.value) rd(0, sm + nxt * G2_STAGE, 0);
    if constexpr (AA.value) { piece(t + 4, cur, 0); piece(t + 4, cur, 1); }
    mm(1); G2_SCHED(AA.value ? 2 : 0);
    cur = nxt;
  };
  using T_ = std::true_type; using F_ = std::false_type;
#pragma unroll 1
  for (int t = 0; t < 12; ++t) step(t, T_{}, T_{}, std::integral_constant<int, 12>{}, T_{});
  step(12, T_{}, F_{}, std::integral_constant<int, 12>{}, T_{});
  step(13, F_{}, F_{}, std::integral_constant<int, 6>{}, T_{});
  step(14, F_{}, F_{}, std::integral_constant<int, 0>{}, T_{});
  step(15, F_{}, F_{}, std::integral_constant<int, 0>{}, F_{});
  ST(2,3);
  char* ot = sm + wave * 4096;
#pragma unroll
  for (int g = 0; g < 2; ++g)
#pragma unroll
    for (int qd = 0; qd < 4; ++qd) {
      u32x2 w = {pk_bf16(acc[g][4 * qd], acc[g][4 * qd + 1]), pk_bf16(acc[g][4 * qd + 2], acc[g][4 * qd + 3])};
      *(u32x2*)(ot + l31 * 128 + (((4 * g + qd) ^ (l31 & 7)) << 4) + h * 8) = w;
    }
  WAIT_L0();
#pragma unroll
  for (int it = 0; it < 4; ++it) {
    const int row = it * 8 + (lane >> 3), c = lane & 7;
    const u32x4 d = *(const u32x4*)(ot + row * 128 + ((c ^ (row & 7)) << 4));
    *(u32x4*)(out + (size_t)(rb * 128 + wave * 32 + row) * 2048 + 1024 + nb * 64 + c * 8) = d;
  }
  ST(2,4);
}

__global__ __launch_bounds__(256, 2) void k3_gemm(const u16* wo, const char* ws, u16* out) {
  extern __shared__ __attribute__((aligned(16))) char sm[];
  role_gemm2(blockIdx.x, sm, wo, ws, out);
}
__global__ __launch_bounds__(256) void k3_a3(const u16* q, const char* ws, u16* out) {
  __shared__ __attribute__((aligned(16))) char sm[16384];
  role_a3(blockIdx.x, sm, q, ws, out);
}

__global__ __launch_bounds__(256) void k0_lead(const char* p0, const char* p1, const char* p2, const char* p3, const char* p4, const char* p5, const char* p6, unsigned* sink) {
  const int b = blockIdx.x; const char* p; int o;
  if (b < 128) { p = p0; o = b; } else if (b < 256) { p = p1; o = b - 128; } else if (b < 384) { p = p2; o = b - 256; } else if (b < 512) { p = p3; o = b - 384; }
  else if (b < 640) { p = p4; o = b - 512; } else if (b < 832) { p = p5; o = b - 640; } else { p = p6; o = b - 832; }
  p += (size_t)o * 32768 + threadIdx.x * 16;
  u32x4 v[8]; unsigned acc = 0;
#pragma unroll
  for (int u = 0; u < 8; ++u) v[u] = *(const u32x4*)(p + u * 4096);
#pragma unroll
  for (int u = 0; u < 8; ++u) acc ^= v[u][0] ^ v[u][3];
  if (acc == 0x1234567u) sink[0] = acc;
}
extern "C" void kernel_launch(void* const* d_in, const int* in_sizes, int n_in, void* d_out, int out_size, void* d_ws, size_t ws_size,
                              hipStream_t stream) {
  const u16 *q = (const u16*)d_in[0], *k = (const u16*)d_in[1], *v = (const u16*)d_in[2], *shk = (const u16*)d_in[3];
  const u16 *hk = (const u16*)d_in[4], *Dv = (const u16*)d_in[5], *x = (const u16*)d_in[6], *W = (const u16*)d_in[7];
  const u16 *wphi = (const u16*)d_in[8], *wo = (const u16*)d_in[9];
  u16* out = (u16*)d_out; char* ws = (char*)d_ws;
  hipFuncSetAttribute((const void*)k1_gemm, hipFuncAttributeMaxDynamicSharedMemorySize, G1_LDS);
  hipFuncSetAttribute((const void*)k2_conv, hipFuncAttributeMaxDynamicSharedMemorySize, CV_LDS);
  hipFuncSetAttribute((const void*)k3_gemm, hipFuncAttributeMaxDynamicSharedMemorySize, G2_LDS);
  k0_lead<<<896, 256, 0, stream>>>((const char*)q, (const char*)k, (const char*)v, (const char*)hk, (const char*)x, (const char*)W, (const char*)wo, (unsigned*)(ws + (100u << 20)));
  k1_a1<<<256, 256, 0, stream>>>(k, v, shk, ws);
  k1_gemm<<<256, 256, G1_LDS, stream>>>(x, W, wphi, ws);
  k2_conv<<<256, 256, CV_LDS, stream>>>(hk, Dv, ws);
  k2_o<<<256, 256, 0, stream>>>(wphi, ws);
  k3_gemm<<<256, 256, G2_LDS, stream>>>(wo, ws, out);
  k3_a3<<<256, 256, 0, stream>>>(q, ws, out);
}
```

```cpp
#include <hip/hip_runtime.h>
#include <stdint.h>
#include <type_traits>

typedef short bf16x8 __attribute__((ext_vector_type(8)));
typedef short s16x4 __attribute__((ext_vector_type(4)));
typedef float f32x4 __attribute__((ext_vector_type(4)));
typedef float f32x2 __attribute__((ext_vector_type(2)));
typedef float f32x16 __attribute__((ext_vector_type(16)));
typedef unsigned u32x4 __attribute__((ext_vector_type(4)));
typedef unsigned u32x2 __attribute__((ext_vector_type(2)));
typedef __bf16 bfx2 __attribute__((ext_vector_type(2)));
typedef unsigned short u16;
#define DI __device__ __forceinline__
#define LAS __attribute__((address_space(3)))

constexpr int S_ = 2048, D_ = 1024;
constexpr size_t WS_XT = 0;
constexpr size_t WS_YT = 4u << 20;
constexpr size_t WS_QF = 8u << 20;
constexpr size_t WS_OF = 12u << 20;
constexpr size_t WS_KVP = 16u << 20;
constexpr size_t WS_ZP = 20u << 20;

DI unsigned pk_bf16(float lo, float hi) { f32x2 p = {lo, hi}; return __builtin_bit_cast(unsigned, __builtin_convertvector(p, bfx2)); }
DI float bf_lo(unsigned u) { return __uint_as_float(u << 16); }
DI float bf_hi(unsigned u) { return __uint_as_float(u & 0xffff0000u); }
DI float rbf(float x) { return bf_lo(pk_bf16(x, 0.f)); }
DI f32x16 mfma32(bf16x8 a, bf16x8 b, f32x16 c) { return __builtin_amdgcn_mfma_f32_32x32x16_bf16(a, b, c, 0, 0, 0); }
DI f32x4 mfma16(bf16x8 a, bf16x8 b, f32x4 c) { return __builtin_amdgcn_mfma_f32_16x16x32_bf16(a, b, c, 0, 0, 0); }
DI s16x4 tr_read(const char* p) { return __builtin_amdgcn_ds_read_tr16_b64_v4i16((LAS s16x4*)p); }
DI bf16x8 cat8(s16x4 a, s16x4 b) { return __builtin_shufflevector(a, b, 0, 1, 2, 3, 4, 5, 6, 7); }
typedef __amdgpu_buffer_rsrc_t rsrc_t;
DI rsrc_t mk_rsrc(const void* p, unsigned bytes) { return __builtin_amdgcn_make_buffer_rsrc((void*)p, 0, bytes, 0x00020000); }
DI void blds16(rsrc_t r, unsigned voff, unsigned soff, void* l) { __builtin_amdgcn_raw_ptr_buffer_load_lds(r, (LAS void*)l, 16, voff, soff, 0, 0); }
DI void glds16(const void* g, void* l) { __builtin_amdgcn_global_load_lds((const unsigned*)g, (unsigned*)l, 16, 0, 0); }
#define WAIT_V(n) asm volatile("s_waitcnt vmcnt(%0)" ::"n"(n) : "memory")
#define WAIT_L0() asm volatile("s_waitcnt lgkmcnt(0)" ::: "memory")
#define BAR() do { WAIT_L0(); __builtin_amdgcn_s_barrier(); asm volatile("" ::: "memory"); } while (0)
#define SCHED0() __builtin_amdgcn_sched_barrier(0)
#define SGB(m, n) __builtin_amdgcn_sched_group_barrier(m, n, 0)
#define G1_SCHED(NV) do { SGB(0x100, 3); SGB(0x8, 1); SGB(0x100, 2); SGB(0x8, 1); SGB(0x100, 3); SGB(0x8, 1); \
    if ((NV) > 0) SGB(0x10, 1); SGB(0x8, 1); if ((NV) > 1) SGB(0x10, 1); SGB(0x8, 1); if ((NV) > 2) SGB(0x10, 1); SGB(0x8, 1); if ((NV) > 3) SGB(0x10, 1); SCHED0(); } while (0)
#define ST(kid, i)
DI float xhalf(float v) { return __shfl_xor(v, 32); }
DI bf16x8 pack8(const f32x16& x, int s) {
  u32x4 p;
  p[0] = pk_bf16(x[8 * s + 0], x[8 * s + 1]); p[1] = pk_bf16(x[8 * s + 2], x[8 * s + 3]);
  p[2] = pk_bf16(x[8 * s + 4], x[8 * s + 5]); p[3] = pk_bf16(x[8 * s + 6], x[8 * s + 7]);
  return __builtin_bit_cast(bf16x8, p);
}
DI void dual_softmax(const f32x16& pin, bf16x8* pos, bf16x8* neg) {
  float p[16];
#pragma unroll
  for (int i = 0; i < 8; ++i) { unsigned u = pk_bf16(pin[2 * i], pin[2 * i + 1]); p[2 * i] = bf_lo(u); p[2 * i + 1] = bf_hi(u); }
  float mx = p[0], mn = p[0];
#pragma unroll
  for (int i = 1; i < 16; ++i) { mx = fmaxf(mx, p[i]); mn = fminf(mn, p[i]); }
  mx = fmaxf(mx, xhalf(mx)); mn = fminf(mn, xhalf(mn));
  const float L2E = 1.4426950408889634f;
  float ep[16], en[16], sp = 0.f, sn = 0.f;
  const float mxl = mx * L2E, mnl = mn * L2E;
#pragma unroll
  for (int i = 0; i < 16; ++i) {
    ep[i] = __builtin_amdgcn_exp2f(fmaf(p[i], L2E, -mxl)); en[i] = __builtin_amdgcn_exp2f(fmaf(p[i], -L2E, mnl));
    sp += ep[i]; sn += en[i];
  }
  sp += xhalf(sp); sn += xhalf(sn);
  const float rp = __builtin_amdgcn_rcpf(sp), rn = __builtin_amdgcn_rcpf(sn);
#pragma unroll
  for (int s = 0; s < 2; ++s) {
    u32x4 a, b;
#pragma unroll
    for (int i = 0; i < 4; ++i) {
      a[i] = pk_bf16(ep[8 * s + 2 * i] * rp, ep[8 * s + 2 * i + 1] * rp);
      b[i] = pk_bf16(en[8 * s + 2 * i] * rn, en[8 * s + 2 * i + 1] * rn);
    }
    pos[s] = __builtin_bit_cast(bf16x8, a); neg[s] = __builtin_bit_cast(bf16x8, b);
  }
}

DI void role_a1(int tile, char* sm, const u16* __restrict__ k, const u16* __restrict__ v, const u16* __restrict__ shk, char* __restrict__ ws) {
  const int tid = threadIdx.x, cg = tile & 15, rb = tile >> 4;
  const int cgp = tid & 7, rg = tid >> 3;
  const int ch0 = cg * 64 + cgp * 8, r0 = rb * 128 + rg * 4;
  u32x4 kk[7], vv[4], sk[4];
#pragma unroll
  for (int i = 0; i < 7; ++i) {
    int r = r0 - 3 + i;
    kk[i] = *(const u32x4*)(k + (size_t)(r < 0 ? 0 : r) * D_ + ch0);
    if (r < 0) kk[i] = u32x4{0u, 0u, 0u, 0u};
  }
#pragma unroll
  for (int i = 0; i < 4; ++i) vv[i] = *(const u32x4*)(v + (size_t)(r0 + i) * D_ + ch0);
#pragma unroll
  for (int i = 0; i < 4; ++i) sk[i] = *(const u32x4*)(shk + ch0 * 4 + i * 8);
#pragma unroll
  for (int e = 0; e < 8; ++e) {
    const unsigned w01 = sk[e >> 1][(e & 1) * 2], w23 = sk[e >> 1][(e & 1) * 2 + 1];
    const float w[4] = {bf_lo(w01), bf_hi(w01), bf_lo(w23), bf_hi(w23)};
    float kf[7];
#pragma unroll
    for (int i = 0; i < 7; ++i) kf[i] = (e & 1) ? bf_hi(kk[i][e >> 1]) : bf_lo(kk[i][e >> 1]);
    float o[4];
#pragma unroll
    for (int i = 0; i < 4; ++i) {
      float a = rbf(kf[i] * w[3]);
      a = rbf(a + rbf(kf[i + 1] * w[2]));
      a = rbf(a + rbf(kf[i + 2] * w[1]));
      a = rbf(a + rbf(kf[i + 3] * w[0]));
      const float vf = (e & 1) ? bf_hi(vv[i][e >> 1]) : bf_lo(vv[i][e >> 1]);
      o[i] = a * vf;
    }
    u32x2 w2 = {pk_bf16(o[0], o[1]), pk_bf16(o[2], o[3])};
    *(u32x2*)(sm + (cgp * 8 + e) * 256 + ((rg ^ (2 * cgp)) << 3)) = w2;
  }
  __syncthreads();
#pragma unroll
  for (int it = 0; it < 4; ++it) {
    const int ch = it * 16 + (tid >> 4), l = tid & 15;
    const int c = ((l & 7) << 1) | (l >> 3);
    u32x4 d = *(const u32x4*)(sm + ch * 256 + ((c ^ (ch >> 3)) << 4));
    const int gc = rb * 16 + c;
    *(u32x4*)(ws + WS_XT + (size_t)(cg * 64 + ch) * 4096 + (gc & 1) * 2048 + (gc >> 1) * 16) = d;
  }
}

constexpr int G1_STAGE = 40960, G1_NBUF = 3, G1_WPHI = G1_STAGE * G1_NBUF;
constexpr int G1_LDS = G1_WPHI + 4096;

DI void role_gemm1(int bid, char* sm, const u16* __restrict__ x, const u16* __restrict__ W, const u16* __restrict__ wphi, char* __restrict__ ws) {
  const int tid = threadIdx.x, lane = tid & 63;
  const int wave = __builtin_amdgcn_readfirstlane(tid >> 6);
  const int xcd = bid & 7, idx = bid >> 3;
  const int rb = (xcd & 1) * 8 + (idx & 7), j = (xcd >> 1) * 4 + (idx >> 3);
  const int wm = wave & 1, wn = wave >> 1;
  const int h = lane >> 5, l31 = lane & 31;
  ST(0,0);
  const rsrc_t rx = mk_rsrc(x, 2048 * 1024 * 2), rw = mk_rsrc(W, 1024 * 3072 * 2), rp = mk_rsrc(wphi, 65536);
  unsigned xs[4], wsrc[6];
#pragma unroll
  for (int i = 0; i < 4; ++i) {
    const int p = wave * 4 + i, row = p * 8 + (lane >> 3), ch = (lane & 7) ^ ((row >> 1) & 7);
    xs[i] = ((rb * 128 + row) * 1024 + ch * 8) * 2;
  }
#pragma unroll
  for (int i = 0; i < 6; ++i) {
    const int p = wave * 6 + i, kg = p / 3, seg = p % 3;
    wsrc[i] = ((kg * 8 + ((lane >> 2) & 7)) * 3072 + seg * 1024 + j * 64 + (lane >> 5) * 32 + (lane & 3) * 8) * 2;
  }
  auto piece = [&](int t, int buf, int i) {
    char* b = sm + buf * G1_STAGE;
    if (i < 4) blds16(rx, xs[i], t * 128, b + (wave * 4 + i) * 1024);
    else blds16(rw, wsrc[i - 4], t * (64 * 3072 * 2), b + 16384 + (wave * 6 + i - 4) * 1024);
  };
  blds16(rp, (j * 2048 + wave * 512 + lane * 8) * 2, 0, sm + G1_WPHI + wave * 1024);
#pragma unroll
  for (int i = 0; i < 10; ++i) piece(0, 0, i);
#pragma unroll
  for (int i = 0; i < 10; ++i) piece(1, 1, i);
#pragma unroll
  for (int i = 0; i < 4; ++i) piece(2, 2, i);
  int xoff[4];
  {
    const int row = wm * 64 + l31, sw = (row >> 1) & 7;
#pragma unroll
    for (int s = 0; s < 4; ++s) xoff[s] = row * 128 + (((2 * s + h) ^ sw) << 4);
  }
  const int blk = (lane >> 4) & 1, q4 = (lane & 15) >> 2, p4 = lane & 3;
  const int woff = 16384 + h * 3072 + wn * 512 + q4 * 64 + (2 * blk + (p4 >> 1)) * 16 + (p4 & 1) * 8;
  f32x16 acc[2][3];
#pragma unroll
  for (int a = 0; a < 2; ++a)
#pragma unroll
    for (int b = 0; b < 3; ++b)
#pragma unroll
      for (int i = 0; i < 16; ++i) acc[a][b][i] = 0.f;
  bf16x8 xf[2][2], wf[2][3];
  auto rd = [&](int set, const char* b, int s) {
    xf[set][0] = *(const bf16x8*)(b + xoff[s]);
#pragma unroll
    for (int g = 0; g < 3; ++g) wf[set][g] = cat8(tr_read(b + woff + s * 6144 + g * 1024), tr_read(b + woff + s * 6144 + g * 1024 + 256));
    xf[set][1] = *(const bf16x8*)(b + xoff[s] + 4096);
  };
  auto mm = [&](int set) {
#pragma unroll
    for (int ts = 0; ts < 2; ++ts) {
      acc[ts][0] = mfma32(wf[set][0], xf[set][ts], acc[ts][0]);
      acc[ts][1] = mfma32(wf[set][1], xf[set][ts], acc[ts][1]);
      acc[ts][2] = mfma32(xf[set][ts], wf[set][2], acc[ts][2]);
    }
  };
  ST(0,1);
  WAIT_V(14); BAR();
  ST(0,2);
  rd(0, sm, 0);
  SCHED0();
  int cur = 0, nxt = 1, nx2 = 2;
  auto step = [&](int t, auto BC, auto AA, auto WN, auto RN) {
    const char* b = sm + cur * G1_STAGE;
    rd(1, b, 1);
    if constexpr (BC.value) { piece(t + 2, nx2, 4); piece(t + 2, nx2, 5); piece(t + 2, nx2, 6); }
    mm(0);
    G1_SCHED(BC.value ? 3 : 0);
    rd(0, b, 2);
    if constexpr (BC.value) { piece(t + 2, nx2, 7); piece(t + 2, nx2, 8); piece(t + 2, nx2, 9); }
    mm(1);
    G1_SCHED(BC.value ? 3 : 0);
    rd(1, b, 3); mm(0);
    G1_SCHED(0);
    WAIT_V(WN.value);
    BAR();
    SCHED0();
    if constexpr (RN.value) rd(0, sm + nxt * G1_STAGE, 0);
    if constexpr (AA.value) { piece(t + 3, cur, 0); piece(t + 3, cur, 1); piece(t + 3, cur, 2); piece(t + 3, cur, 3); }
    mm(1);
    G1_SCHED(AA.value ? 4 : 0);
    const int o = cur; cur = nxt; nxt = nx2; nx2 = o;
  };
  using T_ = std::true_type; using F_ = std::false_type;
#pragma unroll 1
  for (int t = 0; t < 13; ++t) step(t, T_{}, T_{}, std::integral_constant<int, 10>{}, T_{});
  step(13, T_{}, F_{}, std::integral_constant<int, 10>{}, T_{});
  step(14, F_{}, F_{}, std::integral_constant<int, 0>{}, T_{});
  step(15, F_{}, F_{}, std::integral_constant<int, 0>{}, F_{});
  ST(0,3);
  const int head = 2 * j + wn;
#pragma unroll
  for (int ts = 0; ts < 2; ++ts) {
    char* dst = ws + WS_QF + ((size_t)((rb * 4 + wm * 2 + ts) * 32 + head) * 2) * 1024 + lane * 16;
    *(bf16x8*)dst = pack8(acc[ts][0], 0);
    *(bf16x8*)(dst + 1024) = pack8(acc[ts][0], 1);
  }
  bf16x8 wp[2];
  {
    const char* wb = sm + G1_WPHI + wn * 2048 + (4 * h + q4) * 64 + (2 * blk + (p4 >> 1)) * 16 + (p4 & 1) * 8;
#pragma unroll
    for (int s = 0; s < 2; ++s) wp[s] = cat8(tr_read(wb + s * 1024), tr_read(wb + s * 1024 + 512));
  }
  char* img = sm + wave * 8192;
  f32x16 zero;
#pragma unroll
  for (int i = 0; i < 16; ++i) zero[i] = 0.f;
#pragma unroll
  for (int ts = 0; ts < 2; ++ts) {
    f32x16 pk = mfma32(wp[0], pack8(acc[ts][1], 0), zero);
    pk = mfma32(wp[1], pack8(acc[ts][1], 1), pk);
    bf16x8 pos[2], neg[2];
    dual_softmax(pk, pos, neg);
    const int row = ts * 32 + l31, sw = (row >> 1) & 7;
#pragma unroll
    for (int g = 0; g < 4; ++g) {
      const u32x4 pp = __builtin_bit_cast(u32x4, pos[g >> 1]), nn = __builtin_bit_cast(u32x4, neg[g >> 1]);
      u32x2 a = {pp[(g & 1) * 2], pp[(g & 1) * 2 + 1]}, b = {nn[(g & 1) * 2], nn[(g & 1) * 2 + 1]};
      const int o = row * 64 + (((2 * g + h) ^ sw) << 3);
      *(u32x2*)(img + o) = a;
      *(u32x2*)(img + 4096 + o) = b;
    }
  }
  ST(0,4);
  f32x16 kvacc[2], zacc[2];
  kvacc[0] = zero; kvacc[1] = zero; zacc[0] = zero; zacc[1] = zero;
  bf16x8 ones;
#pragma unroll
  for (int i = 0; i < 8; ++i) ones[i] = (short)0x3f80;
#pragma unroll
  for (int ts = 0; ts < 2; ++ts)
#pragma unroll
    for (int s = 0; s < 2; ++s) {
      const bf16x8 vb = pack8(acc[ts][2], s);
      const int r0 = ts * 32 + 16 * s + 4 * h + q4, r1 = r0 + 8;
      const int o0 = r0 * 64 + (((4 * blk + p4) ^ ((r0 >> 1) & 7)) << 3), o1 = r1 * 64 + (((4 * blk + p4) ^ ((r1 >> 1) & 7)) << 3);
#pragma unroll
      for (int tl = 0; tl < 2; ++tl) {
        const bf16x8 pa = cat8(tr_read(img + tl * 4096 + o0), tr_read(img + tl * 4096 + o1));
        kvacc[tl] = mfma32(pa, vb, kvacc[tl]);
        zacc[tl] = mfma32(pa, ones, zacc[tl]);
      }
    }
  ST(0,5);
  char* red = sm + 32768 + wn * 8704;
  if (wm == 1) {
#pragma unroll
    for (int tl = 0; tl < 2; ++tl)
#pragma unroll
      for (int qd = 0; qd < 4; ++qd) {
        f32x4 t = {kvacc[tl][4 * qd], kvacc[tl][4 * qd + 1], kvacc[tl][4 * qd + 2], kvacc[tl][4 * qd + 3]};
        *(f32x4*)(red + (tl * 4 + qd) * 1024 + lane * 16) = t;
        if (l31 == 0) {
          f32x4 z = {zacc[tl][4 * qd], zacc[tl][4 * qd + 1], zacc[tl][4 * qd + 2], zacc[tl][4 * qd + 3]};
          *(f32x4*)(red + 8192 + ((tl * 2 + h) * 4 + qd) * 16) = z;
        }
      }
  }
  BAR();
  if (wm == 0) {
    char* dst = ws + WS_KVP + (size_t)(rb * 32 + head) * 8192 + lane * 16;
    char* zdst = ws + WS_ZP + (size_t)(rb * 32 + head) * 256;
#pragma unroll
    for (int tl = 0; tl < 2; ++tl)
#pragma unroll
      for (int qd = 0; qd < 4; ++qd) {
        f32x4 t = *(const f32x4*)(red + (tl * 4 + qd) * 1024 + lane * 16);
        t[0] += kvacc[tl][4 * qd]; t[1] += kvacc[tl][4 * qd + 1]; t[2] += kvacc[tl][4 * qd + 2]; t[3] += kvacc[tl][4 * qd + 3];
        *(f32x4*)(dst + (tl * 4 + qd) * 1024) = t;
        if (l31 == 0) {
          f32x4 z = *(const f32x4*)(red + 8192 + ((tl * 2 + h) * 4 + qd) * 16);
          z[0] += zacc[tl][4 * qd]; z[1] += zacc[tl][4 * qd + 1]; z[2] += zacc[tl][4 * qd + 2]; z[3] += zacc[tl][4 * qd + 3];
          *(f32x4*)(zdst + ((tl * 2 + h) * 4 + qd) * 16) = z;
        }
      }
  }
  ST(0,6);
}

__global__ __launch_bounds__(256, 2) void k1_gemm(const u16* x, const u16* W, const u16* wphi, char* ws) {
  extern __shared__ __attribute__((aligned(16))) char sm[];
  role_gemm1(blockIdx.x, sm, x, W, wphi, ws);
}
__global__ __launch_bounds__(256) void k1_a1(const u16* k, const u16* v, const u16* shk, char* ws) {
  __shared__ __attribute__((aligned(16))) char sm[16384];
  role_a1(blockIdx.x, sm, k, v, shk, ws);
}

constexpr int CV_WIN = 16512 + 512 + 64, CV_XR = 144 * 16, CV_WAVE = CV_WIN + 2 * CV_XR;
constexpr int CV_LDS = 4 * CV_WAVE;

DI int win_addr(int m0) {
  const int u = m0 + 16;
  return ((u >> 3) << 6) + ((u >> 8) << 6) + ((((u >> 1) & 3) ^ ((u >> 4) & 3)) << 4) + ((u & 1) << 3);
}

DI void role_conv(int bid, char* sm, const u16* __restrict__ hk, const u16* __restrict__ Dv, char* __restrict__ ws) {
  const int tid = threadIdx.x, lane = tid & 63;
  const int wave = __builtin_amdgcn_readfirstlane(tid >> 6);
  const int ch = bid * 4 + wave;
  char* wl = sm + wave * CV_WAVE;
  char* xl = wl + CV_WIN;
  const u16* hrow = hk + (size_t)ch * 2048;
  const char* xsrc = ws + WS_XT + (size_t)ch * 4096;
  ST(1,0);
  u32x4 hv[4], xv[4]; u32x2 hp[4];
#pragma unroll
  for (int b = 0; b < 4; ++b) {
    const int e0 = b * 512 + lane * 8;
    hv[b] = *(const u32x4*)(hrow + e0);
    hp[b] = *(const u32x2*)(hrow + (e0 > 0 ? e0 - 4 : 0));
    xv[b] = *(const u32x4*)(xsrc + b * 1024 + lane * 16);
  }
  const float dcoef = bf_lo((unsigned)Dv[ch]);
  if (lane == 0) { hp[0][0] = 0u; hp[0][1] = 0u; }
  {
    u32x4 z = {0u, 0u, 0u, 0u};
    if (lane < 8) *(u32x4*)(wl + lane * 16) = z;
    if (lane < 32) *(u32x4*)(xl + (lane >> 4) * CV_XR + (lane & 15) * 16) = z;
  }
#pragma unroll
  for (int b = 0; b < 4; ++b) {
    *(u32x4*)(xl + (b >> 1) * CV_XR + 256 + ((b & 1) * 64 + lane) * 16) = xv[b];
    const unsigned dm2 = hp[b][0], dm1 = hp[b][1], d0 = hv[b][0], d1 = hv[b][1], d2 = hv[b][2], d3 = hv[b][3];
    unsigned R[10];
    R[0] = __builtin_amdgcn_perm(dm2, dm1, 0x07060100u);
    R[1] = __builtin_amdgcn_alignbit(dm1, dm1, 16);
    R[2] = __builtin_amdgcn_perm(dm1, d0, 0x07060100u);
    R[3] = __builtin_amdgcn_alignbit(d0, d0, 16);
    R[4] = __builtin_amdgcn_perm(d0, d1, 0x07060100u);
    R[5] = __builtin_amdgcn_alignbit(d1, d1, 16);
    R[6] = __builtin_amdgcn_perm(d1, d2, 0x07060100u);
    R[7] = __builtin_amdgcn_alignbit(d2, d2, 16);
    R[8] = __builtin_amdgcn_perm(d2, d3, 0x07060100u);
    R[9] = __builtin_amdgcn_alignbit(d3, d3, 16);
    const int i = b * 64 + lane + 2;
    const int sw = (i >> 1) & 3;
#pragma unroll
    for (int r = 0; r < 4; ++r) {
      u32x4 w = {R[2 * r + 2], R[2 * r], R[2 * r + 3], R[2 * r + 1]};
      *(u32x4*)(wl + i * 64 + ((i >> 5) << 6) + ((r ^ sw) << 4)) = w;
    }
  }
  ST(1,1);
  const int a = lane & 15, g = lane >> 4;
  const int off = (g & 1) * 16 + (g >> 1) * 8;
  const int cb = 2 * a + 1 - (off >> 3);
  const int xb0 = CV_WIN + (cb & 1) * CV_XR + (((cb >> 1) + 16) << 4);
  f32x4 acc[8];
#pragma unroll
  for (int c = 0; c < 8; ++c) acc[c] = f32x4{0.f, 0.f, 0.f, 0.f};
  bf16x8 A[2][8], B[2][8];
  auto ld = [&](int set, int t0) {
    const int m0 = 32 * t0 + a + off - 8;
    const char* pa0 = wl + win_addr(m0);
    const char* pa1 = wl + win_addr(m0 - 4);
    const char* pb = wl + xb0 - t0 * 32;
#pragma unroll
    for (int i = 0; i < 8; ++i) {
      A[set][i] = cat8(*(const s16x4*)(pa0 + i * 2112), *(const s16x4*)(pa1 + i * 2112));
      B[set][i] = *(const bf16x8*)(pb + i * 256);
    }
  };
  auto mm = [&](int set) {
#pragma unroll
    for (int t1 = 0; t1 < 8; ++t1)
#pragma unroll
      for (int i = 0; i + t1 < 8; ++i) acc[t1 + i] = mfma16(A[set][t1], B[set][i], acc[t1 + i]);
  };
  ld(0, 0);
  SCHED0();
#define CV_SCHED() do { _Pragma("unroll") for (int z = 0; z < 24; ++z) { SGB(0x8, 1); SGB(0x100, 1); } SGB(0x8, 12); SCHED0(); } while (0)
#pragma unroll 1
  for (int t0 = 0; t0 < 6; t0 += 2) {
    ld(1, t0 + 1); mm(0); CV_SCHED();
    ld(0, t0 + 2); mm(1); CV_SCHED();
  }
  ld(1, 7); mm(0); CV_SCHED();
  mm(1);
  ST(1,2);
  char* ydst = ws + WS_YT + (size_t)ch * 4096 + (16 * a + 4 * g) * 2;
  const int xe = CV_WIN + (g >> 1) * CV_XR + ((a + 16) << 4) + (g & 1) * 8;
#pragma unroll
  for (int c = 0; c < 8; ++c) {
    const u32x2 kv = *(const u32x2*)(wl + xe + c * 256);
    const unsigned y01 = pk_bf16(acc[c][0], acc[c][1]), y23 = pk_bf16(acc[c][2], acc[c][3]);
    const unsigned t01 = pk_bf16(bf_lo(kv[0]) * dcoef, bf_hi(kv[0]) * dcoef), t23 = pk_bf16(bf_lo(kv[1]) * dcoef, bf_hi(kv[1]) * dcoef);
    u32x2 o = {pk_bf16(bf_lo(y01) + bf_lo(t01), bf_hi(y01) + bf_hi(t01)), pk_bf16(bf_lo(y23) + bf_lo(t23), bf_hi(y23) + bf_hi(t23))};
    *(u32x2*)(ydst + c * 512) = o;
  }
  ST(1,3);
}

DI void role_o(int bid, char* sm, const u16* __restrict__ wphi, char* __restrict__ ws) {
  const int tid = threadIdx.x, lane = tid & 63, wave = tid >> 6;
  const int head = (bid & 7) * 4 + ((bid >> 3) & 3), tb = bid >> 5;
  const int h = lane >> 5;
  const int blk = (lane >> 4) & 1, q4 = (lane & 15) >> 2, p4 = lane & 3;
  bf16x8 qf[2][2];
#pragma unroll
  for (int ts = 0; ts < 2; ++ts) {
    const char* src = ws + WS_QF + ((size_t)((tb * 8 + wave * 2 + ts) * 32 + head) * 2) * 1024 + lane * 16;
    qf[ts][0] = *(const bf16x8*)src; qf[ts][1] = *(const bf16x8*)(src + 1024);
  }
  if (tid < 128) *(u32x4*)(sm + 8192 + tid * 16) = *(const u32x4*)(wphi + head * 1024 + tid * 8);
  {
    const char* src = ws + WS_KVP + (size_t)head * 8192 + tid * 16;
    f32x4 s0 = {0.f, 0.f, 0.f, 0.f}, s1 = s0, e0[16], e1[16];
#pragma unroll
    for (int r = 0; r < 16; ++r) { e0[r] = *(const f32x4*)(src + (size_t)r * 32 * 8192); e1[r] = *(const f32x4*)(src + (size_t)r * 32 * 8192 + 4096); }
    float zs = 0.f, ze[16];
    if (tid < 64) {
#pragma unroll
      for (int r = 0; r < 16; ++r) ze[r] = *(const float*)(ws + WS_ZP + (size_t)(r * 32 + head) * 256 + tid * 4);
#pragma unroll
      for (int r = 0; r < 16; ++r) zs += ze[r];
    }
#pragma unroll
    for (int r = 0; r < 16; ++r) { s0 += e0[r]; s1 += e1[r]; }
    const int qd = tid >> 6;
    u32x2 a = {pk_bf16(s0[0], s0[1]), pk_bf16(s0[2], s0[3])}, b = {pk_bf16(s1[0], s1[1]), pk_bf16(s1[2], s1[3])};
    *(u32x2*)(sm + (qd >> 1) * 1024 + lane * 16 + (qd & 1) * 8) = a;
    *(u32x2*)(sm + 2048 + (qd >> 1) * 1024 + lane * 16 + (qd & 1) * 8) = b;
    if (tid < 64) *(u16*)(sm + 4096 + tid * 2) = (u16)(pk_bf16(zs, 0.f) & 0xffffu);
  }
  __syncthreads();
  bf16x8 kvf[4], zf[4], wp[2];
#pragma unroll
  for (int i = 0; i < 4; ++i) {
    kvf[i] = *(const bf16x8*)(sm + i * 1024 + lane * 16);
    zf[i] = *(const bf16x8*)(sm + 4096 + ((i >> 1) * 2 + h) * 32 + (i & 1) * 16);
  }
  {
    const char* wb = sm + 8192 + (4 * h + q4) * 64 + (2 * blk + (p4 >> 1)) * 16 + (p4 & 1) * 8;
#pragma unroll
    for (int s = 0; s < 2; ++s) wp[s] = cat8(tr_read(wb + s * 1024), tr_read(wb + s * 1024 + 512));
  }
  f32x16 zero;
#pragma unroll
  for (int i = 0; i < 16; ++i) zero[i] = 0.f;
#pragma unroll
  for (int ts = 0; ts < 2; ++ts) {
    f32x16 pq = mfma32(wp[0], qf[ts][0], zero);
    pq = mfma32(wp[1], qf[ts][1], pq);
    bf16x8 ph[4];
    dual_softmax(pq, ph, ph + 2);
    f32x16 num = zero, den = zero;
#pragma unroll
    for (int i = 0; i < 4; ++i) { num = mfma32(kvf[i], ph[i], num); den = mfma32(zf[i], ph[i], den); }
    const float dn = rbf(rbf(den[0]) + 1e-6f);
    const float rd = 1.0f / dn;
    f32x16 o;
#pragma unroll
    for (int i = 0; i < 8; ++i) {
      const unsigned u = pk_bf16(num[2 * i], num[2 * i + 1]);
      o[2 * i] = bf_lo(u) * rd; o[2 * i + 1] = bf_hi(u) * rd;
    }
    char* dst = ws + WS_OF + ((size_t)((tb * 8 + wave * 2 + ts) * 32 + head) * 2) * 1024 + lane * 16;
    *(bf16x8*)dst = pack8(o, 0);
    *(bf16x8*)(dst + 1024) = pack8(o, 1);
  }
}

__global__ __launch_bounds__(256, 2) void k2_conv(const u16* hk, const u16* Dv, char* ws) {
  extern __shared__ __attribute__((aligned(16))) char sm[];
  role_conv(blockIdx.x, sm, hk, Dv, ws);
}
__global__ __launch_bounds__(256, 2) void k2_o(const u16* wphi, char* ws) {
  __shared__ __attribute__((aligned(16))) char sm[10240];
  role_o(blockIdx.x, sm, wphi, ws);
}

DI void role_a3(int tile, char* sm, const u16* __restrict__ q, const char* __restrict__ ws, u16* __restrict__ out) {
  const int tid = threadIdx.x, cg = tile & 15, rb = tile >> 4;
  const int cgp = tid & 7, rg = tid >> 3;
  const int ch0 = cg * 64 + cgp * 8, r0 = rb * 128 + rg * 4;
  u32x4 qq[4], yy[4];
#pragma unroll
  for (int it = 0; it < 4; ++it) {
    const int ch = it * 16 + (tid >> 4), c = tid & 15;
    yy[it] = *(const u32x4*)(ws + WS_YT + (size_t)(cg * 64 + ch) * 4096 + rb * 256 + c * 16);
  }
#pragma unroll
  for (int i = 0; i < 4; ++i) qq[i] = *(const u32x4*)(q + (size_t)(r0 + i) * D_ + ch0);
#pragma unroll
  for (int it = 0; it < 4; ++it) {
    const int ch = it * 16 + (tid >> 4), c = tid & 15;
    *(u32x4*)(sm + ch * 256 + ((c ^ (ch >> 3)) << 4)) = yy[it];
  }
  __syncthreads();
  u32x2 y[8];
#pragma unroll
  for (int e = 0; e < 8; ++e) y[e] = *(const u32x2*)(sm + (cgp * 8 + e) * 256 + ((rg ^ (2 * cgp)) << 3));
#pragma unroll
  for (int i = 0; i < 4; ++i) {
    u32x4 o;
#pragma unroll
    for (int e2 = 0; e2 < 4; ++e2) {
      const unsigned ya = y[2 * e2][i >> 1], yb = y[2 * e2 + 1][i >> 1];
      const float fa = (i & 1) ? bf_hi(ya) : bf_lo(ya), fb = (i & 1) ? bf_hi(yb) : bf_lo(yb);
      o[e2] = pk_bf16(bf_lo(qq[i][e2]) * fa, bf_hi(qq[i][e2]) * fb);
    }
    *(u32x4*)(out + (size_t)(r0 + i) * 2048 + ch0) = o;
  }
}

constexpr int G2_STAGE = 24576, G2_NBUF = 4, G2_LDS = G2_STAGE * G2_NBUF;

DI void role_gemm2(int bid, char* sm, const u16* __restrict__ wo, const char* __restrict__ ws, u16* __restrict__ out) {
  const int tid = threadIdx.x, lane = tid & 63;
  const int wave = __builtin_amdgcn_readfirstlane(tid >> 6);
  const int xcd = bid & 7, idx = bid >> 3;
  const int rb = (xcd & 1) * 8 + (idx & 7), nb = (xcd >> 1) * 4 + (idx >> 3);
  const int h = lane >> 5, l31 = lane & 31;
  ST(2,0);
  const rsrc_t ro = mk_rsrc(ws + WS_OF, 4u << 20), rw = mk_rsrc(wo, 2u << 20);
  const unsigned osrc = (rb * 4 + wave) * 65536 + lane * 16;
  const unsigned wsrc = ((wave * 16 + ((lane >> 2) & 7)) * 1024 + nb * 64 + (lane >> 5) * 32 + (lane & 3) * 8) * 2;
  auto piece = [&](int t, int buf, int i) {
    char* b = sm + buf * G2_STAGE;
    if (i < 4) blds16(ro, osrc + i * 1024, t * 4096, b + wave * 4096 + i * 1024);
    else blds16(rw, wsrc + (i - 4) * 16384, t * 131072, b + 16384 + (wave * 2 + i - 4) * 1024);
  };
#pragma unroll
  for (int t = 0; t < 3; ++t)
#pragma unroll
    for (int i = 0; i < 6; ++i) piece(t, t, i);
  piece(3, 3, 0); piece(3, 3, 1);
  const int blk = (lane >> 4) & 1, q4 = (lane & 15) >> 2, p4 = lane & 3;
  const int woff = 16384 + (4 * h + q4) * 64 + (2 * blk + (p4 >> 1)) * 16 + (p4 & 1) * 8;
  const int ooff = wave * 4096 + lane * 16;
  f32x16 acc[2];
#pragma unroll
  for (int b = 0; b < 2; ++b)
#pragma unroll
    for (int i = 0; i < 16; ++i) acc[b][i] = 0.f;
  bf16x8 of[2], wf[2][2];
  auto rd = [&](int set, const char* b, int s) {
    of[set] = *(const bf16x8*)(b + ooff + s * 1024);
#pragma unroll
    for (int g = 0; g < 2; ++g) wf[set][g] = cat8(tr_read(b + woff + s * 2048 + g * 512), tr_read(b + woff + s * 2048 + 1024 + g * 512));
  };
  auto mm = [&](int set) {
    acc[0] = mfma32(wf[set][0], of[set], acc[0]);
    acc[1] = mfma32(wf[set][1], of[set], acc[1]);
  };
  ST(2,1);
  WAIT_V(14); BAR();
  ST(2,2);
  rd(0, sm, 0);
  SCHED0();
  int cur = 0;
#define G2_SCHED(NV) do { SGB(0x100, 3); SGB(0x8, 1); SGB(0x100, 2); if ((NV) > 0) SGB(0x10, 1); SGB(0x8, 1); if ((NV) > 1) SGB(0x10, 1); SCHED0(); } while (0)
  auto step = [&](int t, auto BC, auto AA, auto WN, auto RN) {
    const char* b = sm + cur * G2_STAGE;
    const int nxt = (cur + 1) & 3, prv = (cur + 3) & 3;
    rd(1, b, 1);
    if constexpr (BC.value) { piece(t + 3, prv, 2); piece(t + 3, prv, 3); }
    mm(0); G2_SCHED(BC.value ? 2 : 0);
    rd(0, b, 2);
    if constexpr (BC.value) { piece(t + 3, prv, 4); piece(t + 3, prv, 5); }
    mm(1); G2_SCHED(BC.value ? 2 : 0);
    rd(1, b, 3); mm(0); G2_SCHED(0);
    WAIT_V(WN.value);
    BAR();
    SCHED0();
    if constexpr (RN.value) rd(0, sm + nxt * G2_STAGE, 0);
    if constexpr (AA.value) { piece(t + 4, cur, 0); piece(t + 4, cur, 1); }
    mm(1); G2_SCHED(AA.value ? 2 : 0);
    cur = nxt;
  };
  using T_ = std::true_type; using F_ = std::false_type;
#pragma unroll 1
  for (int t = 0; t < 12; ++t) step(t, T_{}, T_{}, std::integral_constant<int, 12>{}, T_{});
  step(12, T_{}, F_{}, std::integral_constant<int, 12>{}, T_{});
  step(13, F_{}, F_{}, std::integral_constant<int, 6>{}, T_{});
  step(14, F_{}, F_{}, std::integral_constant<int, 0>{}, T_{});
  step(15, F_{}, F_{}, std::integral_constant<int, 0>{}, F_{});
  ST(2,3);
  char* ot = sm + wave * 4096;
#pragma unroll
  for (int g = 0; g < 2; ++g)
#pragma unroll
    for (int qd = 0; qd < 4; ++qd) {
      u32x2 w = {pk_bf16(acc[g][4 * qd], acc[g][4 * qd + 1]), pk_bf16(acc[g][4 * qd + 2], acc[g][4 * qd + 3])};
      *(u32x2*)(ot + l31 * 128 + (((4 * g + qd) ^ (l31 & 7)) << 4) + h * 8) = w;
    }
  WAIT_L0();
#pragma unroll
  for (int it = 0; it < 4; ++it) {
    const int row = it * 8 + (lane >> 3), c = lane & 7;
    const u32x4 d = *(const u32x4*)(ot + row * 128 + ((c ^ (row & 7)) << 4));
    *(u32x4*)(out + (size_t)(rb * 128 + wave * 32 + row) * 2048 + 1024 + nb * 64 + c * 8) = d;
  }
  ST(2,4);
}

__global__ __launch_bounds__(256, 2) void k3_gemm(const u16* wo, const char* ws, u16* out) {
  extern __shared__ __attribute__((aligned(16))) char sm[];
  role_gemm2(blockIdx.x, sm, wo, ws, out);
}
__global__ __launch_bounds__(256) void k3_a3(const u16* q, const char* ws, u16* out) {
  __shared__ __attribute__((aligned(16))) char sm[16384];
  role_a3(blockIdx.x, sm, q, ws, out);
}

__global__ __launch_bounds__(256) void k0_lead(const char* p0, const char* p1, const char* p2, const char* p3, const char* p4, const char* p5, const char* p6, unsigned* sink) {
  unsigned long long r0 = __builtin_amdgcn_s_memrealtime();
  const int b = blockIdx.x; const char* p; int o;
  if (b < 128) { p = p0; o = b; } else if (b < 256) { p = p1; o = b - 128; } else if (b < 384) { p = p2; o = b - 256; } else if (b < 512) { p = p3; o = b - 384; }
  else if (b < 640) { p = p4; o = b - 512; } else if (b < 832) { p = p5; o = b - 640; } else { p = p6; o = b - 832; }
  p += (size_t)o * 32768 + threadIdx.x * 16;
  u32x4 v[8]; unsigned acc = 0;
#pragma unroll
  for (int u = 0; u < 8; ++u) v[u] = *(const u32x4*)(p + u * 4096);
#pragma unroll
  for (int u = 0; u < 8; ++u) acc ^= v[u][0] ^ v[u][3];
  if (acc == 0x1234567u) sink[0] = acc;
  __syncthreads();
  unsigned long long r1 = __builtin_amdgcn_s_memrealtime();
  if (threadIdx.x == 0) { atomicMin((unsigned long long*)(sink + 16), r0); atomicMax((unsigned long long*)(sink + 32), r1); }
}
__global__ void k_init(unsigned* sink) { *(unsigned long long*)(sink + 16) = ~0ull; *(unsigned long long*)(sink + 32) = 0ull; }
__global__ void k_spin(unsigned* sink) {
  unsigned long long T = *(unsigned long long*)(sink + 32) - *(unsigned long long*)(sink + 16);
  if (T > 10000) T = 10000;
  unsigned long long r0 = __builtin_amdgcn_s_memrealtime();
  while (__builtin_amdgcn_s_memrealtime() - r0 < T * 10) __builtin_amdgcn_s_sleep(1);
}
extern "C" void kernel_launch(void* const* d_in, const int* in_sizes, int n_in, void* d_out, int out_size, void* d_ws, size_t ws_size,
                              hipStream_t stream) {
  const u16 *q = (const u16*)d_in[0], *k = (const u16*)d_in[1], *v = (const u16*)d_in[2], *shk = (const u16*)d_in[3];
  const u16 *hk = (const u16*)d_in[4], *Dv = (const u16*)d_in[5], *x = (const u16*)d_in[6], *W = (const u16*)d_in[7];
  const u16 *wphi = (const u16*)d_in[8], *wo = (const u16*)d_in[9];
  u16* out = (u16*)d_out; char* ws = (char*)d_ws;
  hipFuncSetAttribute((const void*)k1_gemm, hipFuncAttributeMaxDynamicSharedMemorySize, G1_LDS);
  hipFuncSetAttribute((const void*)k2_conv, hipFuncAttributeMaxDynamicSharedMemorySize, CV_LDS);
  hipFuncSetAttribute((const void*)k3_gemm, hipFuncAttributeMaxDynamicSharedMemorySize, G2_LDS);
  k_init<<<1, 1, 0, stream>>>((unsigned*)(ws + (100u << 20)));
  k0_lead<<<896, 256, 0, stream>>>((const char*)q, (const char*)k, (const char*)v, (const char*)hk, (const char*)x, (const char*)W, (const char*)wo, (unsigned*)(ws + (100u << 20)));
  k1_a1<<<256, 256, 0, stream>>>(k, v, shk, ws);
  k1_gemm<<<256, 256, G1_LDS, stream>>>(x, W, wphi, ws);
  k2_conv<<<256, 256, CV_LDS, stream>>>(hk, Dv, ws);
  k2_o<<<256, 256, 0, stream>>>(wphi, ws);
  k3_gemm<<<256, 256, G2_LDS, stream>>>(wo, ws, out);
  k3_a3<<<256, 256, 0, stream>>>(q, ws, out);
  k_spin<<<1, 64, 0, stream>>>((unsigned*)(ws + (100u << 20)));
}
```

```cpp
#include <hip/hip_runtime.h>
#include <stdint.h>
#include <type_traits>

typedef short bf16x8 __attribute__((ext_vector_type(8)));
typedef short s16x4 __attribute__((ext_vector_type(4)));
typedef float f32x4 __attribute__((ext_vector_type(4)));
typedef float f32x2 __attribute__((ext_vector_type(2)));
typedef float f32x16 __attribute__((ext_vector_type(16)));
typedef unsigned u32x4 __attribute__((ext_vector_type(4)));
typedef unsigned u32x2 __attribute__((ext_vector_type(2)));
typedef __bf16 bfx2 __attribute__((ext_vector_type(2)));
typedef unsigned short u16;
#define DI __device__ __forceinline__
#define LAS __attribute__((address_space(3)))

constexpr int S_ = 2048, D_ = 1024;
constexpr size_t WS_XT = 0;
constexpr size_t WS_YT = 4u << 20;
constexpr size_t WS_QF = 8u << 20;
constexpr size_t WS_OF = 12u << 20;
constexpr size_t WS_KVP = 16u << 20;
constexpr size_t WS_ZP = 20u << 20;

DI unsigned pk_bf16(float lo, float hi) { f32x2 p = {lo, hi}; return __builtin_bit_cast(unsigned, __builtin_convertvector(p, bfx2)); }
DI float bf_lo(unsigned u) { return __uint_as_float(u << 16); }
DI float bf_hi(unsigned u) { return __uint_as_float(u & 0xffff0000u); }
DI float rbf(float x) { return bf_lo(pk_bf16(x, 0.f)); }
DI f32x16 mfma32(bf16x8 a, bf16x8 b, f32x16 c) { return __builtin_amdgcn_mfma_f32_32x32x16_bf16(a, b, c, 0, 0, 0); }
DI f32x4 mfma16(bf16x8 a, bf16x8 b, f32x4 c) { return __builtin_amdgcn_mfma_f32_16x16x32_bf16(a, b, c, 0, 0, 0); }
DI s16x4 tr_read(const char* p) { return __builtin_amdgcn_ds_read_tr16_b64_v4i16((LAS s16x4*)p); }
DI bf16x8 cat8(s16x4 a, s16x4 b) { return __builtin_shufflevector(a, b, 0, 1, 2, 3, 4, 5, 6, 7); }
typedef __amdgpu_buffer_rsrc_t rsrc_t;
DI rsrc_t mk_rsrc(const void* p, unsigned bytes) { return __builtin_amdgcn_make_buffer_rsrc((void*)p, 0, bytes, 0x00020000); }
DI void blds16(rsrc_t r, unsigned voff, unsigned soff, void* l) { __builtin_amdgcn_raw_ptr_buffer_load_lds(r, (LAS void*)l, 16, voff, soff, 0, 0); }
DI void glds16(const void* g, void* l) { __builtin_amdgcn_global_load_lds((const unsigned*)g, (unsigned*)l, 16, 0, 0); }
#define WAIT_V(n) asm volatile("s_waitcnt vmcnt(%0)" ::"n"(n) : "memory")
#define WAIT_L0() asm volatile("s_waitcnt lgkmcnt(0)" ::: "memory")
#define BAR() do { WAIT_L0(); __builtin_amdgcn_s_barrier(); asm volatile("" ::: "memory"); } while (0)
#define SCHED0() __builtin_amdgcn_sched_barrier(0)
#define SGB(m, n) __builtin_amdgcn_sched_group_barrier(m, n, 0)
#define G1_SCHED(NV) do { SGB(0x100, 3); SGB(0x8, 1); SGB(0x100, 2); SGB(0x8, 1); SGB(0x100, 3); SGB(0x8, 1); \
    if ((NV) > 0) SGB(0x10, 1); SGB(0x8, 1); if ((NV) > 1) SGB(0x10, 1); SGB(0x8, 1); if ((NV) > 2) SGB(0x10, 1); SGB(0x8, 1); if ((NV) > 3) SGB(0x10, 1); SCHED0(); } while (0)
#define ST(kid, i)
DI float xhalf(float v) { return __shfl_xor(v, 32); }
DI bf16x8 pack8(const f32x16& x, int s) {
  u32x4 p;
  p[0] = pk_bf16(x[8 * s + 0], x[8 * s + 1]); p[1] = pk_bf16(x[8 * s + 2], x[8 * s + 3]);
  p[2] = pk_bf16(x[8 * s + 4], x[8 * s + 5]); p[3] = pk_bf16(x[8 * s + 6], x[8 * s + 7]);
  return __builtin_bit_cast(bf16x8, p);
}
DI void dual_softmax(const f32x16& pin, bf16x8* pos, bf16x8* neg) {
  float p[16];
#pragma unroll
  for (int i = 0; i < 8; ++i) { unsigned u = pk_bf16(pin[2 * i], pin[2 * i + 1]); p[2 * i] = bf_lo(u); p[2 * i + 1] = bf_hi(u); }
  float mx = p[0], mn = p[0];
#pragma unroll
  for (int i = 1; i < 16; ++i) { mx = fmaxf(mx, p[i]); mn = fminf(mn, p[i]); }
  mx = fmaxf(mx, xhalf(mx)); mn = fminf(mn, xhalf(mn));
  const float L2E = 1.4426950408889634f;
  float ep[16], en[16], sp = 0.f, sn = 0.f;
  const float mxl = mx * L2E, mnl = mn * L2E;
#pragma unroll
  for (int i = 0; i < 16; ++i) {
    ep[i] = __builtin_amdgcn_exp2f(fmaf(p[i], L2E, -mxl)); en[i] = __builtin_amdgcn_exp2f(fmaf(p[i], -L2E, mnl));
    sp += ep[i]; sn += en[i];
  }
  sp += xhalf(sp); sn += xhalf(sn);
  const float rp = __builtin_amdgcn_rcpf(sp), rn = __builtin_amdgcn_rcpf(sn);
#pragma unroll
  for (int s = 0; s < 2; ++s) {
    u32x4 a, b;
#pragma unroll
    for (int i = 0; i < 4; ++i) {
      a[i] = pk_bf16(ep[8 * s + 2 * i] * rp, ep[8 * s + 2 * i + 1] * rp);
      b[i] = pk_bf16(en[8 * s + 2 * i] * rn, en[8 * s + 2 * i + 1] * rn);
    }
    pos[s] = __builtin_bit_cast(bf16x8, a); neg[s] = __builtin_bit_cast(bf16x8, b);
  }
}

DI void role_a1(int tile, char* sm, const u16* __restrict__ k, const u16* __restrict__ v, const u16* __restrict__ shk, char* __restrict__ ws) {
  const int tid = threadIdx.x, cg = tile & 15, rb = tile >> 4;
  const int cgp = tid & 7, rg = tid >> 3;
  const int ch0 = cg * 64 + cgp * 8, r0 = rb * 128 + rg * 4;
  u32x4 kk[7], vv[4], sk[4];
#pragma unroll
  for (int i = 0; i < 7; ++i) {
    int r = r0 - 3 + i;
    kk[i] = *(const u32x4*)(k + (size_t)(r < 0 ? 0 : r) * D_ + ch0);
    if (r < 0) kk[i] = u32x4{0u, 0u, 0u, 0u};
  }
#pragma unroll
  for (int i = 0; i < 4; ++i) vv[i] = *(const u32x4*)(v + (size_t)(r0 + i) * D_ + ch0);
#pragma unroll
  for (int i = 0; i < 4; ++i) sk[i] = *(const u32x4*)(shk + ch0 * 4 + i * 8);
#pragma unroll
  for (int e = 0; e < 8; ++e) {
    const unsigned w01 = sk[e >> 1][(e & 1) * 2], w23 = sk[e >> 1][(e & 1) * 2 + 1];
    const float w[4] = {bf_lo(w01), bf_hi(w01), bf_lo(w23), bf_hi(w23)};
    float kf[7];
#pragma unroll
    for (int i = 0; i < 7; ++i) kf[i] = (e & 1) ? bf_hi(kk[i][e >> 1]) : bf_lo(kk[i][e >> 1]);
    float o[4];
#pragma unroll
    for (int i = 0; i < 4; ++i) {
      float a = rbf(kf[i] * w[3]);
      a = rbf(a + rbf(kf[i + 1] * w[2]));
      a = rbf(a + rbf(kf[i + 2] * w[1]));
      a = rbf(a + rbf(kf[i + 3] * w[0]));
      const float vf = (e & 1) ? bf_hi(vv[i][e >> 1]) : bf_lo(vv[i][e >> 1]);
      o[i] = a * vf;
    }
    u32x2 w2 = {pk_bf16(o[0], o[1]), pk_bf16(o[2], o[3])};
    *(u32x2*)(sm + (cgp * 8 + e) * 256 + ((rg ^ (2 * cgp)) << 3)) = w2;
  }
  __syncthreads();
#pragma unroll
  for (int it = 0; it < 4; ++it) {
    const int ch = it * 16 + (tid >> 4), l = tid & 15;
    const int c = ((l & 7) << 1) | (l >> 3);
    u32x4 d = *(const u32x4*)(sm + ch * 256 + ((c ^ (ch >> 3)) << 4));
    const int gc = rb * 16 + c;
    *(u32x4*)(ws + WS_XT + (size_t)(cg * 64 + ch) * 4096 + (gc & 1) * 2048 + (gc >> 1) * 16) = d;
  }
}

constexpr int G1_STAGE = 40960, G1_NBUF = 3, G1_WPHI = G1_STAGE * G1_NBUF;
constexpr int G1_LDS = G1_WPHI + 4096;

DI void role_gemm1(int bid, char* sm, const u16* __restrict__ x, const u16* __restrict__ W, const u16* __restrict__ wphi, char* __restrict__ ws) {
  const int tid = threadIdx.x, lane = tid & 63;
  const int wave = __builtin_amdgcn_readfirstlane(tid >> 6);
  const int xcd = bid & 7, idx = bid >> 3;
  const int rb = (xcd & 1) * 8 + (idx & 7), j = (xcd >> 1) * 4 + (idx >> 3);
  const int wm = wave & 1, wn = wave >> 1;
  const int h = lane >> 5, l31 = lane & 31;
  ST(0,0);
  const rsrc_t rx = mk_rsrc(x, 2048 * 1024 * 2), rw = mk_rsrc(W, 1024 * 3072 * 2), rp = mk_rsrc(wphi, 65536);
  unsigned xs[4], wsrc[6];
#pragma unroll
  for (int i = 0; i < 4; ++i) {
    const int p = wave * 4 + i, row = p * 8 + (lane >> 3), ch = (lane & 7) ^ ((row >> 1) & 7);
    xs[i] = ((rb * 128 + row) * 1024 + ch * 8) * 2;
  }
#pragma unroll
  for (int i = 0; i < 6; ++i) {
    const int p = wave * 6 + i, kg = p / 3, seg = p % 3;
    wsrc[i] = ((kg * 8 + ((lane >> 2) & 7)) * 3072 + seg * 1024 + j * 64 + (lane >> 5) * 32 + (lane & 3) * 8) * 2;
  }
  auto piece = [&](int t, int buf, int i) {
    char* b = sm + buf * G1_STAGE;
    if (i < 4) blds16(rx, xs[i], t * 128, b + (wave * 4 + i) * 1024);
    else blds16(rw, wsrc[i - 4], t * (64 * 3072 * 2), b + 16384 + (wave * 6 + i - 4) * 1024);
  };
  blds16(rp, (j * 2048 + wave * 512 + lane * 8) * 2, 0, sm + G1_WPHI + wave * 1024);
#pragma unroll
  for (int i = 0; i < 10; ++i) piece(0, 0, i);
#pragma unroll
  for (int i = 0; i < 10; ++i) piece(1, 1, i);
#pragma unroll
  for (int i = 0; i < 4; ++i) piece(2, 2, i);
  int xoff[4];
  {
    const int row = wm * 64 + l31, sw = (row >> 1) & 7;
#pragma unroll
    for (int s = 0; s < 4; ++s) xoff[s] = row * 128 + (((2 * s + h) ^ sw) << 4);
  }
  const int blk = (lane >> 4) & 1, q4 = (lane & 15) >> 2, p4 = lane & 3;
  const int woff = 16384 + h * 3072 + wn * 512 + q4 * 64 + (2 * blk + (p4 >> 1)) * 16 + (p4 & 1) * 8;
  f32x16 acc[2][3];
#pragma unroll
  for (int a = 0; a < 2; ++a)
#pragma unroll
    for (int b = 0; b < 3; ++b)
#pragma unroll
      for (int i = 0; i < 16; ++i) acc[a][b][i] = 0.f;
  bf16x8 xf[2][2], wf[2][3];
  auto rd = [&](int set, const char* b, int s) {
    xf[set][0] = *(const bf16x8*)(b + xoff[s]);
#pragma unroll
    for (int g = 0; g < 3; ++g) wf[set][g] = cat8(tr_read(b + woff + s * 6144 + g * 1024), tr_read(b + woff + s * 6144 + g * 1024 + 256));
    xf[set][1] = *(const bf16x8*)(b + xoff[s] + 4096);
  };
  auto mm = [&](int set) {
#pragma unroll
    for (int ts = 0; ts < 2; ++ts) {
      acc[ts][0] = mfma32(wf[set][0], xf[set][ts], acc[ts][0]);
      acc[ts][1] = mfma32(wf[set][1], xf[set][ts], acc[ts][1]);
      acc[ts][2] = mfma32(xf[set][ts], wf[set][2], acc[ts][2]);
    }
  };
  ST(0,1);
  WAIT_V(14); BAR();
  ST(0,2);
  rd(0, sm, 0);
  SCHED0();
  int cur = 0, nxt = 1, nx2 = 2;
  auto step = [&](int t, auto BC, auto AA, auto WN, auto RN) {
    const char* b = sm + cur * G1_STAGE;
    rd(1, b, 1);
    if constexpr (BC.value) { piece(t + 2, nx2, 4); piece(t + 2, nx2, 5); piece(t + 2, nx2, 6); }
    mm(0);
    G1_SCHED(BC.value ? 3 : 0);
    rd(0, b, 2);
    if constexpr (BC.value) { piece(t + 2, nx2, 7); piece(t + 2, nx2, 8); piece(t + 2, nx2, 9); }
    mm(1);
    G1_SCHED(BC.value ? 3 : 0);
    rd(1, b, 3); mm(0);
    G1_SCHED(0);
    WAIT_V(WN.value);
    BAR();
    SCHED0();
    if constexpr (RN.value) rd(0, sm + nxt * G1_STAGE, 0);
    if constexpr (AA.value) { piece(t + 3, cur, 0); piece(t + 3, cur, 1); piece(t + 3, cur, 2); piece(t + 3, cur, 3); }
    mm(1);
    G1_SCHED(AA.value ? 4 : 0);
    const int o = cur; cur = nxt; nxt = nx2; nx2 = o;
  };
  using T_ = std::true_type; using F_ = std::false_type;
#pragma unroll 1
  for (int t = 0; t < 13; ++t) step(t, T_{}, T_{}, std::integral_constant<int, 10>{}, T_{});
  step(13, T_{}, F_{}, std::integral_constant<int, 10>{}, T_{});
  step(14, F_{}, F_{}, std::integral_constant<int, 0>{}, T_{});
  step(15, F_{}, F_{}, std::integral_constant<int, 0>{}, F_{});
  ST(0,3);
  const int head = 2 * j + wn;
#pragma unroll
  for (int ts = 0; ts < 2; ++ts) {
    char* dst = ws + WS_QF + ((size_t)((rb * 4 + wm * 2 + ts) * 32 + head) * 2) * 1024 + lane * 16;
    *(bf16x8*)dst = pack8(acc[ts][0], 0);
    *(bf16x8*)(dst + 1024) = pack8(acc[ts][0], 1);
  }
  bf16x8 wp[2];
  {
    const char* wb = sm + G1_WPHI + wn * 2048 + (4 * h + q4) * 64 + (2 * blk + (p4 >> 1)) * 16 + (p4 & 1) * 8;
#pragma unroll
    for (int s = 0; s < 2; ++s) wp[s] = cat8(tr_read(wb + s * 1024), tr_read(wb + s * 1024 + 512));
  }
  char* img = sm + wave * 8192;
  f32x16 zero;
#pragma unroll
  for (int i = 0; i < 16; ++i) zero[i] = 0.f;
#pragma unroll
  for (int ts = 0; ts < 2; ++ts) {
    f32x16 pk = mfma32(wp[0], pack8(acc[ts][1], 0), zero);
    pk = mfma32(wp[1], pack8(acc[ts][1], 1), pk);
    bf16x8 pos[2], neg[2];
    dual_softmax(pk, pos, neg);
    const int row = ts * 32 + l31, sw = (row >> 1) & 7;
#pragma unroll
    for (int g = 0; g < 4; ++g) {
      const u32x4 pp = __builtin_bit_cast(u32x4, pos[g >> 1]), nn = __builtin_bit_cast(u32x4, neg[g >> 1]);
      u32x2 a = {pp[(g & 1) * 2], pp[(g & 1) * 2 + 1]}, b = {nn[(g & 1) * 2], nn[(g & 1) * 2 + 1]};
      const int o = row * 64 + (((2 * g + h) ^ sw) << 3);
      *(u32x2*)(img + o) = a;
      *(u32x2*)(img + 4096 + o) = b;
    }
  }
  ST(0,4);
  f32x16 kvacc[2], zacc[2];
  kvacc[0] = zero; kvacc[1] = zero; zacc[0] = zero; zacc[1] = zero;
  bf16x8 ones;
#pragma unroll
  for (int i = 0; i < 8; ++i) ones[i] = (short)0x3f80;
#pragma unroll
  for (int ts = 0; ts < 2; ++ts)
#pragma unroll
    for (int s = 0; s < 2; ++s) {
      const bf16x8 vb = pack8(acc[ts][2], s);
      const int r0 = ts * 32 + 16 * s + 4 * h + q4, r1 = r0 + 8;
      const int o0 = r0 * 64 + (((4 * blk + p4) ^ ((r0 >> 1) & 7)) << 3), o1 = r1 * 64 + (((4 * blk + p4) ^ ((r1 >> 1) & 7)) << 3);
#pragma unroll
      for (int tl = 0; tl < 2; ++tl) {
        const bf16x8 pa = cat8(tr_read(img + tl * 4096 + o0), tr_read(img + tl * 4096 + o1));
        kvacc[tl] = mfma32(pa, vb, kvacc[tl]);
        zacc[tl] = mfma32(pa, ones, zacc[tl]);
      }
    }
  ST(0,5);
  char* red = sm + 32768 + wn * 8704;
  if (wm == 1) {
#pragma unroll
    for (int tl = 0; tl < 2; ++tl)
#pragma unroll
      for (int qd = 0; qd < 4; ++qd) {
        f32x4 t = {kvacc[tl][4 * qd], kvacc[tl][4 * qd + 1], kvacc[tl][4 * qd + 2], kvacc[tl][4 * qd + 3]};
        *(f32x4*)(red + (tl * 4 + qd) * 1024 + lane * 16) = t;
        if (l31 == 0) {
          f32x4 z = {zacc[tl][4 * qd], zacc[tl][4 * qd + 1], zacc[tl][4 * qd + 2], zacc[tl][4 * qd + 3]};
          *(f32x4*)(red + 8192 + ((tl * 2 + h) * 4 + qd) * 16) = z;
        }
      }
  }
  BAR();
  if (wm == 0) {
    char* dst = ws + WS_KVP + (size_t)(rb * 32 + head) * 8192 + lane * 16;
    char* zdst = ws + WS_ZP + (size_t)(rb * 32 + head) * 256;
#pragma unroll
    for (int tl = 0; tl < 2; ++tl)
#pragma unroll
      for (int qd = 0; qd < 4; ++qd) {
        f32x4 t = *(const f32x4*)(red + (tl * 4 + qd) * 1024 + lane * 16);
        t[0] += kvacc[tl][4 * qd]; t[1] += kvacc[tl][4 * qd + 1]; t[2] += kvacc[tl][4 * qd + 2]; t[3] += kvacc[tl][4 * qd + 3];
        *(f32x4*)(dst + (tl * 4 + qd) * 1024) = t;
        if (l31 == 0) {
          f32x4 z = *(const f32x4*)(red + 8192 + ((tl * 2 + h) * 4 + qd) * 16);
          z[0] += zacc[tl][4 * qd]; z[1] += zacc[tl][4 * qd + 1]; z[2] += zacc[tl][4 * qd + 2]; z[3] += zacc[tl][4 * qd + 3];
          *(f32x4*)(zdst + ((tl * 2 + h) * 4 + qd) * 16) = z;
        }
      }
  }
  ST(0,6);
}

__global__ __launch_bounds__(256, 2) void k1_gemm(const u16* x, const u16* W, const u16* wphi, char* ws) {
  extern __shared__ __attribute__((aligned(16))) char sm[];
  role_gemm1(blockIdx.x, sm, x, W, wphi, ws);
}
__global__ __launch_bounds__(256) void k1_a1(const u16* k, const u16* v, const u16* shk, char* ws) {
  __shared__ __attribute__((aligned(16))) char sm[16384];
  role_a1(blockIdx.x, sm, k, v, shk, ws);
}

constexpr int CV_WIN = 16512 + 512 + 64, CV_XR = 144 * 16, CV_WAVE = CV_WIN + 2 * CV_XR;
constexpr int CV_LDS = 4 * CV_WAVE;

DI int win_addr(int m0) {
  const int u = m0 + 16;
  return ((u >> 3) << 6) + ((u >> 8) << 6) + ((((u >> 1) & 3) ^ ((u >> 4) & 3)) << 4) + ((u & 1) << 3);
}

DI void role_conv(int bid, char* sm, const u16* __restrict__ hk, const u16* __restrict__ Dv, char* __restrict__ ws) {
  const int tid = threadIdx.x, lane = tid & 63;
  const int wave = __builtin_amdgcn_readfirstlane(tid >> 6);
  const int ch = bid * 4 + wave;
  char* wl = sm + wave * CV_WAVE;
  char* xl = wl + CV_WIN;
  const u16* hrow = hk + (size_t)ch * 2048;
  const char* xsrc = ws + WS_XT + (size_t)ch * 4096;
  ST(1,0);
  u32x4 hv[4], xv[4]; u32x2 hp[4];
#pragma unroll
  for (int b = 0; b < 4; ++b) {
    const int e0 = b * 512 + lane * 8;
    hv[b] = *(const u32x4*)(hrow + e0);
    hp[b] = *(const u32x2*)(hrow + (e0 > 0 ? e0 - 4 : 0));
    xv[b] = *(const u32x4*)(xsrc + b * 1024 + lane * 16);
  }
  const float dcoef = bf_lo((unsigned)Dv[ch]);
  if (lane == 0) { hp[0][0] = 0u; hp[0][1] = 0u; }
  {
    u32x4 z = {0u, 0u, 0u, 0u};
    if (lane < 8) *(u32x4*)(wl + lane * 16) = z;
    if (lane < 32) *(u32x4*)(xl + (lane >> 4) * CV_XR + (lane & 15) * 16) = z;
  }
#pragma unroll
  for (int b = 0; b < 4; ++b) {
    *(u32x4*)(xl + (b >> 1) * CV_XR + 256 + ((b & 1) * 64 + lane) * 16) = xv[b];
    const unsigned dm2 = hp[b][0], dm1 = hp[b][1], d0 = hv[b][0], d1 = hv[b][1], d2 = hv[b][2], d3 = hv[b][3];
    unsigned R[10];
    R[0] = __builtin_amdgcn_perm(dm2, dm1, 0x07060100u);
    R[1] = __builtin_amdgcn_alignbit(dm1, dm1, 16);
    R[2] = __builtin_amdgcn_perm(dm1, d0, 0x07060100u);
    R[3] = __builtin_amdgcn_alignbit(d0, d0, 16);
    R[4] = __builtin_amdgcn_perm(d0, d1, 0x07060100u);
    R[5] = __builtin_amdgcn_alignbit(d1, d1, 16);
    R[6] = __builtin_amdgcn_perm(d1, d2, 0x07060100u);
    R[7] = __builtin_amdgcn_alignbit(d2, d2, 16);
    R[8] = __builtin_amdgcn_perm(d2, d3, 0x07060100u);
    R[9] = __builtin_amdgcn_alignbit(d3, d3, 16);
    const int i = b * 64 + lane + 2;
    const int sw = (i >> 1) & 3;
#pragma unroll
    for (int r = 0; r < 4; ++r) {
      u32x4 w = {R[2 * r + 2], R[2 * r], R[2 * r + 3], R[2 * r + 1]};
      *(u32x4*)(wl + i * 64 + ((i >> 5) << 6) + ((r ^ sw) << 4)) = w;
    }
  }
  ST(1,1);
  const int a = lane & 15, g = lane >> 4;
  const int off = (g & 1) * 16 + (g >> 1) * 8;
  const int cb = 2 * a + 1 - (off >> 3);
  const int xb0 = CV_WIN + (cb & 1) * CV_XR + (((cb >> 1) + 16) << 4);
  f32x4 acc[8];
#pragma unroll
  for (int c = 0; c < 8; ++c) acc[c] = f32x4{0.f, 0.f, 0.f, 0.f};
  bf16x8 A[2][8], B[2][8];
  auto ld = [&](int set, int t0) {
    const int m0 = 32 * t0 + a + off - 8;
    const char* pa0 = wl + win_addr(m0);
    const char* pa1 = wl + win_addr(m0 - 4);
    const char* pb = wl + xb0 - t0 * 32;
#pragma unroll
    for (int i = 0; i < 8; ++i) {
      A[set][i] = cat8(*(const s16x4*)(pa0 + i * 2112), *(const s16x4*)(pa1 + i * 2112));
      B[set][i] = *(const bf16x8*)(pb + i * 256);
    }
  };
  auto mm = [&](int set) {
#pragma unroll
    for (int t1 = 0; t1 < 8; ++t1)
#pragma unroll
      for (int i = 0; i + t1 < 8; ++i) acc[t1 + i] = mfma16(A[set][t1], B[set][i], acc[t1 + i]);
  };
  ld(0, 0);
  SCHED0();
#define CV_SCHED() do { _Pragma("unroll") for (int z = 0; z < 24; ++z) { SGB(0x8, 1); SGB(0x100, 1); } SGB(0x8, 12); SCHED0(); } while (0)
#pragma unroll 1
  for (int t0 = 0; t0 < 6; t0 += 2) {
    ld(1, t0 + 1); mm(0); CV_SCHED();
    ld(0, t0 + 2); mm(1); CV_SCHED();
  }
  ld(1, 7); mm(0); CV_SCHED();
  mm(1);
  ST(1,2);
  char* ydst = ws + WS_YT + (size_t)ch * 4096 + (16 * a + 4 * g) * 2;
  const int xe = CV_WIN + (g >> 1) * CV_XR + ((a + 16) << 4) + (g & 1) * 8;
#pragma unroll
  for (int c = 0; c < 8; ++c) {
    const u32x2 kv = *(const u32x2*)(wl + xe + c * 256);
    const unsigned y01 = pk_bf16(acc[c][0], acc[c][1]), y23 = pk_bf16(acc[c][2], acc[c][3]);
    const unsigned t01 = pk_bf16(bf_lo(kv[0]) * dcoef, bf_hi(kv[0]) * dcoef), t23 = pk_bf16(bf_lo(kv[1]) * dcoef, bf_hi(kv[1]) * dcoef);
    u32x2 o = {pk_bf16(bf_lo(y01) + bf_lo(t01), bf_hi(y01) + bf_hi(t01)), pk_bf16(bf_lo(y23) + bf_lo(t23), bf_hi(y23) + bf_hi(t23))};
    *(u32x2*)(ydst + c * 512) = o;
  }
  ST(1,3);
}

DI void role_o(int bid, char* sm, const u16* __restrict__ wphi, char* __restrict__ ws) {
  const int tid = threadIdx.x, lane = tid & 63, wave = tid >> 6;
  const int head = (bid & 7) * 4 + ((bid >> 3) & 3), tb = bid >> 5;
  const int h = lane >> 5;
  const int blk = (lane >> 4) & 1, q4 = (lane & 15) >> 2, p4 = lane & 3;
  bf16x8 qf[2][2];
#pragma unroll
  for (int ts = 0; ts < 2; ++ts) {
    const char* src = ws + WS_QF + ((size_t)((tb * 8 + wave * 2 + ts) * 32 + head) * 2) * 1024 + lane * 16;
    qf[ts][0] = *(const bf16x8*)src; qf[ts][1] = *(const bf16x8*)(src + 1024);
  }
  if (tid < 128) *(u32x4*)(sm + 8192 + tid * 16) = *(const u32x4*)(wphi + head * 1024 + tid * 8);
  {
    const char* src = ws + WS_KVP + (size_t)head * 8192 + tid * 16;
    f32x4 s0 = {0.f, 0.f, 0.f, 0.f}, s1 = s0, e0[16], e1[16];
#pragma unroll
    for (int r = 0; r < 16; ++r) { e0[r] = *(const f32x4*)(src + (size_t)r * 32 * 8192); e1[r] = *(const f32x4*)(src + (size_t)r * 32 * 8192 + 4096); }
    float zs = 0.f, ze[16];
    if (tid < 64) {
#pragma unroll
      for (int r = 0; r < 16; ++r) ze[r] = *(const float*)(ws + WS_ZP + (size_t)(r * 32 + head) * 256 + tid * 4);
#pragma unroll
      for (int r = 0; r < 16; ++r) zs += ze[r];
    }
#pragma unroll
    for (int r = 0; r < 16; ++r) { s0 += e0[r]; s1 += e1[r]; }
    const int qd = tid >> 6;
    u32x2 a = {pk_bf16(s0[0], s0[1]), pk_bf16(s0[2], s0[3])}, b = {pk_bf16(s1[0], s1[1]), pk_bf16(s1[2], s1[3])};
    *(u32x2*)(sm + (qd >> 1) * 1024 + lane * 16 + (qd & 1) * 8) = a;
    *(u32x2*)(sm + 2048 + (qd >> 1) * 1024 + lane * 16 + (qd & 1) * 8) = b;
    if (tid < 64) *(u16*)(sm + 4096 + tid * 2) = (u16)(pk_bf16(zs, 0.f) & 0xffffu);
  }
  __syncthreads();
  bf16x8 kvf[4], zf[4], wp[2];
#pragma unroll
  for (int i = 0; i < 4; ++i) {
    kvf[i] = *(const bf16x8*)(sm + i * 1024 + lane * 16);
    zf[i] = *(const bf16x8*)(sm + 4096 + ((i >> 1) * 2 + h) * 32 + (i & 1) * 16);
  }
  {
    const char* wb = sm + 8192 + (4 * h + q4) * 64 + (2 * blk + (p4 >> 1)) * 16 + (p4 & 1) * 8;
#pragma unroll
    for (int s = 0; s < 2; ++s) wp[s] = cat8(tr_read(wb + s * 1024), tr_read(wb + s * 1024 + 512));
  }
  f32x16 zero;
#pragma unroll
  for (int i = 0; i < 16; ++i) zero[i] = 0.f;
#pragma unroll
  for (int ts = 0; ts < 2; ++ts) {
    f32x16 pq = mfma32(wp[0], qf[ts][0], zero);
    pq = mfma32(wp[1], qf[ts][1], pq);
    bf16x8 ph[4];
    dual_softmax(pq, ph, ph + 2);
    f32x16 num = zero, den = zero;
#pragma unroll
    for (int i = 0; i < 4; ++i) { num = mfma32(kvf[i], ph[i], num); den = mfma32(zf[i], ph[i], den); }
    const float dn = rbf(rbf(den[0]) + 1e-6f);
    const float rd = 1.0f / dn;
    f32x16 o;
#pragma unroll
    for (int i = 0; i < 8; ++i) {
      const unsigned u = pk_bf16(num[2 * i], num[2 * i + 1]);
      o[2 * i] = bf_lo(u) * rd; o[2 * i + 1] = bf_hi(u) * rd;
    }
    char* dst = ws + WS_OF + ((size_t)((tb * 8 + wave * 2 + ts) * 32 + head) * 2) * 1024 + lane * 16;
    *(bf16x8*)dst = pack8(o, 0);
    *(bf16x8*)(dst + 1024) = pack8(o, 1);
  }
}

__global__ __launch_bounds__(256, 2) void k2_conv(const u16* hk, const u16* Dv, char* ws) {
  extern __shared__ __attribute__((aligned(16))) char sm[];
  role_conv(blockIdx.x, sm, hk, Dv, ws);
}
__global__ __launch_bounds__(256, 2) void k2_o(const u16* wphi, char* ws) {
  __shared__ __attribute__((aligned(16))) char sm[10240];
  role_o(blockIdx.x, sm, wphi, ws);
}

DI void role_a3(int tile, char* sm, const u16* __restrict__ q, const char* __restrict__ ws, u16* __restrict__ out) {
  const int tid = threadIdx.x, cg = tile & 15, rb = tile >> 4;
  const int cgp = tid & 7, rg = tid >> 3;
  const int ch0 = cg * 64 + cgp * 8, r0 = rb * 128 + rg * 4;
  u32x4 qq[4], yy[4];
#pragma unroll
  for (int it = 0; it < 4; ++it) {
    const int ch = it * 16 + (tid >> 4), c = tid & 15;
    yy[it] = *(const u32x4*)(ws + WS_YT + (size_t)(cg * 64 + ch) * 4096 + rb * 256 + c * 16);
  }
#pragma unroll
  for (int i = 0; i < 4; ++i) qq[i] = *(const u32x4*)(q + (size_t)(r0 + i) * D_ + ch0);
#pragma unroll
  for (int it = 0; it < 4; ++it) {
    const int ch = it * 16 + (tid >> 4), c = tid & 15;
    *(u32x4*)(sm + ch * 256 + ((c ^ (ch >> 3)) << 4)) = yy[it];
  }
  __syncthreads();
  u32x2 y[8];
#pragma unroll
  for (int e = 0; e < 8; ++e) y[e] = *(const u32x2*)(sm + (cgp * 8 + e) * 256 + ((rg ^ (2 * cgp)) << 3));
#pragma unroll
  for (int i = 0; i < 4; ++i) {
    u32x4 o;
#pragma unroll
    for (int e2 = 0; e2 < 4; ++e2) {
      const unsigned ya = y[2 * e2][i >> 1], yb = y[2 * e2 + 1][i >> 1];
      const float fa = (i & 1) ? bf_hi(ya) : bf_lo(ya), fb = (i & 1) ? bf_hi(yb) : bf_lo(yb);
      o[e2] = pk_bf16(bf_lo(qq[i][e2]) * fa, bf_hi(qq[i][e2]) * fb);
    }
    *(u32x4*)(out + (size_t)(r0 + i) * 2048 + ch0) = o;
  }
}

constexpr int G2_STAGE = 24576, G2_NBUF = 4, G2_LDS = G2_STAGE * G2_NBUF;

DI void role_gemm2(int bid, char* sm, const u16* __restrict__ wo, const char* __restrict__ ws, u16* __restrict__ out) {
  const int tid = threadIdx.x, lane = tid & 63;
  const int wave = __builtin_amdgcn_readfirstlane(tid >> 6);
  const int xcd = bid & 7, idx = bid >> 3;
  const int rb = (xcd & 1) * 8 + (idx & 7), nb = (xcd >> 1) * 4 + (idx >> 3);
  const int h = lane >> 5, l31 = lane & 31;
  ST(2,0);
  const rsrc_t ro = mk_rsrc(ws + WS_OF, 4u << 20), rw = mk_rsrc(wo, 2u << 20);
  const unsigned osrc = (rb * 4 + wave) * 65536 + lane * 16;
  const unsigned wsrc = ((wave * 16 + ((lane >> 2) & 7)) * 1024 + nb * 64 + (lane >> 5) * 32 + (lane & 3) * 8) * 2;
  auto piece = [&](int t, int buf, int i) {
    char* b = sm + buf * G2_STAGE;
    if (i < 4) blds16(ro, osrc + i * 1024, t * 4096, b + wave * 4096 + i * 1024);
    else blds16(rw, wsrc + (i - 4) * 16384, t * 131072, b + 16384 + (wave * 2 + i - 4) * 1024);
  };
#pragma unroll
  for (int t = 0; t < 3; ++t)
#pragma unroll
    for (int i = 0; i < 6; ++i) piece(t, t, i);
  piece(3, 3, 0); piece(3, 3, 1);
  const int blk = (lane >> 4) & 1, q4 = (lane & 15) >> 2, p4 = lane & 3;
  const int woff = 16384 + (4 * h + q4) * 64 + (2 * blk + (p4 >> 1)) * 16 + (p4 & 1) * 8;
  const int ooff = wave * 4096 + lane * 16;
  f32x16 acc[2];
#pragma unroll
  for (int b = 0; b < 2; ++b)
#pragma unroll
    for (int i = 0; i < 16; ++i) acc[b][i] = 0.f;
  bf16x8 of[2], wf[2][2];
  auto rd = [&](int set, const char* b, int s) {
    of[set] = *(const bf16x8*)(b + ooff + s * 1024);
#pragma unroll
    for (int g = 0; g < 2; ++g) wf[set][g] = cat8(tr_read(b + woff + s * 2048 + g * 512), tr_read(b + woff + s * 2048 + 1024 + g * 512));
  };
  auto mm = [&](int set) {
    acc[0] = mfma32(wf[set][0], of[set], acc[0]);
    acc[1] = mfma32(wf[set][1], of[set], acc[1]);
  };
  ST(2,1);
  WAIT_V(14); BAR();
  ST(2,2);
  rd(0, sm, 0);
  SCHED0();
  int cur = 0;
#define G2_SCHED(NV) do { SGB(0x100, 3); SGB(0x8, 1); SGB(0x100, 2); if ((NV) > 0) SGB(0x10, 1); SGB(0x8, 1); if ((NV) > 1) SGB(0x10, 1); SCHED0(); } while (0)
  auto step = [&](int t, auto BC, auto AA, auto WN, auto RN) {
    const char* b = sm + cur * G2_STAGE;
    const int nxt = (cur + 1) & 3, prv = (cur + 3) & 3;
    rd(1, b, 1);
    if constexpr (BC.value) { piece(t + 3, prv, 2); piece(t + 3, prv, 3); }
    mm(0); G2_SCHED(BC.value ? 2 : 0);
    rd(0, b, 2);
    if constexpr (BC.value) { piece(t + 3, prv, 4); piece(t + 3, prv, 5); }
    mm(1); G2_SCHED(BC.value ? 2 : 0);
    rd(1, b, 3); mm(0); G2_SCHED(0);
    WAIT_V(WN.value);
    BAR();
    SCHED0();
    if constexpr (RN.value) rd(0, sm + nxt * G2_STAGE, 0);
    if constexpr (AA.value) { piece(t + 4, cur, 0); piece(t + 4, cur, 1); }
    mm(1); G2_SCHED(AA.value ? 2 : 0);
    cur = nxt;
  };
  using T_ = std::true_type; using F_ = std::false_type;
#pragma unroll 1
  for (int t = 0; t < 12; ++t) step(t, T_{}, T_{}, std::integral_constant<int, 12>{}, T_{});
  step(12, T_{}, F_{}, std::integral_constant<int, 12>{}, T_{});
  step(13, F_{}, F_{}, std::integral_constant<int, 6>{}, T_{});
  step(14, F_{}, F_{}, std::integral_constant<int, 0>{}, T_{});
  step(15, F_{}, F_{}, std::integral_constant<int, 0>{}, F_{});
  ST(2,3);
  char* ot = sm + wave * 4096;
#pragma unroll
  for (int g = 0; g < 2; ++g)
#pragma unroll
    for (int qd = 0; qd < 4; ++qd) {
      u32x2 w = {pk_bf16(acc[g][4 * qd], acc[g][4 * qd + 1]), pk_bf16(acc[g][4 * qd + 2], acc[g][4 * qd + 3])};
      *(u32x2*)(ot + l31 * 128 + (((4 * g + qd) ^ (l31 & 7)) << 4) + h * 8) = w;
    }
  WAIT_L0();
#pragma unroll
  for (int it = 0; it < 4; ++it) {
    const int row = it * 8 + (lane >> 3), c = lane & 7;
    const u32x4 d = *(const u32x4*)(ot + row * 128 + ((c ^ (row & 7)) << 4));
    *(u32x4*)(out + (size_t)(rb * 128 + wave * 32 + row) * 2048 + 1024 + nb * 64 + c * 8) = d;
  }
  ST(2,4);
}

__global__ __launch_bounds__(256, 2) void k3_gemm(const u16* wo, const char* ws, u16* out) {
  extern __shared__ __attribute__((aligned(16))) char sm[];
  role_gemm2(blockIdx.x, sm, wo, ws, out);
}
__global__ __launch_bounds__(256) void k3_a3(const u16* q, const char* ws, u16* out) {
  __shared__ __attribute__((aligned(16))) char sm[16384];
  role_a3(blockIdx.x, sm, q, ws, out);
}

__global__ __launch_bounds__(256) void k0_lead(const char* p0, const char* p1, const char* p2, const char* p3, const char* p4, const char* p5, const char* p6, unsigned* sink) {
  unsigned long long r0 = __builtin_amdgcn_s_memrealtime();
  const int b = blockIdx.x; const char* p; int o;
  if (b < 128) { p = p0; o = b; } else if (b < 256) { p = p1; o = b - 128; } else if (b < 384) { p = p2; o = b - 256; } else if (b < 512) { p = p3; o = b - 384; }
  else if (b < 640) { p = p4; o = b - 512; } else if (b < 832) { p = p5; o = b - 640; } else { p = p6; o = b - 832; }
  p += (size_t)o * 32768 + threadIdx.x * 16;
  u32x4 v[8]; unsigned acc = 0;
#pragma unroll
  for (int u = 0; u < 8; ++u) v[u] = *(const u32x4*)(p + u * 4096);
#pragma unroll
  for (int u = 0; u < 8; ++u) acc ^= v[u][0] ^ v[u][3];
  if (acc == 0x1234567u) sink[0] = acc;
  __syncthreads();
  unsigned long long r1 = __builtin_amdgcn_s_memrealtime();
  if (threadIdx.x == 0) { ((unsigned long long*)(sink + 64))[2 * b] = r0; ((unsigned long long*)(sink + 64))[2 * b + 1] = r1; }
}
__global__ void k_spin(unsigned* sink) {
  unsigned long long mn = ~0ull, mx = 0;
  for (int i = threadIdx.x; i < 896; i += 64) { unsigned long long a = ((unsigned long long*)(sink + 64))[2 * i], c = ((unsigned long long*)(sink + 64))[2 * i + 1]; mn = a < mn ? a : mn; mx = c > mx ? c : mx; }
  for (int o = 32; o; o >>= 1) { unsigned long long a = __shfl_xor(mn, o), c = __shfl_xor(mx, o); mn = a < mn ? a : mn; mx = c > mx ? c : mx; }
  unsigned long long T = mx - mn;
  if (T > 10000) T = 10000;
  unsigned long long r0 = __builtin_amdgcn_s_memrealtime();
  while (__builtin_amdgcn_s_memrealtime() - r0 < T * 10) __builtin_amdgcn_s_sleep(1);
}
extern "C" void kernel_launch(void* const* d_in, const int* in_sizes, int n_in, void* d_out, int out_size, void* d_ws, size_t ws_size,
                              hipStream_t stream) {
  const u16 *q = (const u16*)d_in[0], *k = (const u16*)d_in[1], *v = (const u16*)d_in[2], *shk = (const u16*)d_in[3];
  const u16 *hk = (const u16*)d_in[4], *Dv = (const u16*)d_in[5], *x = (const u16*)d_in[6], *W = (const u16*)d_in[7];
  const u16 *wphi = (const u16*)d_in[8], *wo = (const u16*)d_in[9];
  u16* out = (u16*)d_out; char* ws = (char*)d_ws;
  hipFuncSetAttribute((const void*)k1_gemm, hipFuncAttributeMaxDynamicSharedMemorySize, G1_LDS);
  hipFuncSetAttribute((const void*)k2_conv, hipFuncAttributeMaxDynamicSharedMemorySize, CV_LDS);
  hipFuncSetAttribute((const void*)k3_gemm, hipFuncAttributeMaxDynamicSharedMemorySize, G2_LDS);
  k0_lead<<<896, 256, 0, stream>>>((const char*)q, (const char*)k, (const char*)v, (const char*)hk, (const char*)x, (const char*)W, (const char*)wo, (unsigned*)(ws + (100u << 20)));
  k1_a1<<<256, 256, 0, stream>>>(k, v, shk, ws);
  k1_gemm<<<256, 256, G1_LDS, stream>>>(x, W, wphi, ws);
  k2_conv<<<256, 256, CV_LDS, stream>>>(hk, Dv, ws);
  k2_o<<<256, 256, 0, stream>>>(wphi, ws);
  k3_gemm<<<256, 256, G2_LDS, stream>>>(wo, ws, out);
  k3_a3<<<256, 256, 0, stream>>>(q, ws, out);
  k_spin<<<1, 64, 0, stream>>>((unsigned*)(ws + (100u << 20)));
}
```

```cpp
#define STAMP
#define MEAS
#define KA 0
#define IA 0
#define AMIN 1
#define KB 0
#define IB 0
#define BMAX 0
#include <hip/hip_runtime.h>
#include <stdint.h>
#include <type_traits>

typedef short bf16x8 __attribute__((ext_vector_type(8)));
typedef short s16x4 __attribute__((ext_vector_type(4)));
typedef float f32x4 __attribute__((ext_vector_type(4)));
typedef float f32x2 __attribute__((ext_vector_type(2)));
typedef float f32x16 __attribute__((ext_vector_type(16)));
typedef unsigned u32x4 __attribute__((ext_vector_type(4)));
typedef unsigned u32x2 __attribute__((ext_vector_type(2)));
typedef __bf16 bfx2 __attribute__((ext_vector_type(2)));
typedef unsigned short u16;
#define DI __device__ __forceinline__
#define LAS __attribute__((address_space(3)))

constexpr int S_ = 2048, D_ = 1024;
constexpr size_t WS_XT = 0;
constexpr size_t WS_YT = 4u << 20;
constexpr size_t WS_QF = 8u << 20;
constexpr size_t WS_OF = 12u << 20;
constexpr size_t WS_KVP = 16u << 20;
constexpr size_t WS_ZP = 20u << 20;

DI unsigned pk_bf16(float lo, float hi) { f32x2 p = {lo, hi}; return __builtin_bit_cast(unsigned, __builtin_convertvector(p, bfx2)); }
DI float bf_lo(unsigned u) { return __uint_as_float(u << 16); }
DI float bf_hi(unsigned u) { return __uint_as_float(u & 0xffff0000u); }
DI float rbf(float x) { return bf_lo(pk_bf16(x, 0.f)); }
DI f32x16 mfma32(bf16x8 a, bf16x8 b, f32x16 c) { return __builtin_amdgcn_mfma_f32_32x32x16_bf16(a, b, c, 0, 0, 0); }
DI f32x4 mfma16(bf16x8 a, bf16x8 b, f32x4 c) { return __builtin_amdgcn_mfma_f32_16x16x32_bf16(a, b, c, 0, 0, 0); }
DI s16x4 tr_read(const char* p) { return __builtin_amdgcn_ds_read_tr16_b64_v4i16((LAS s16x4*)p); }
DI bf16x8 cat8(s16x4 a, s16x4 b) { return __builtin_shufflevector(a, b, 0, 1, 2, 3, 4, 5, 6, 7); }
typedef __amdgpu_buffer_rsrc_t rsrc_t;
DI rsrc_t mk_rsrc(const void* p, unsigned bytes) { return __builtin_amdgcn_make_buffer_rsrc((void*)p, 0, bytes, 0x00020000); }
DI void blds16(rsrc_t r, unsigned voff, unsigned soff, void* l) { __builtin_amdgcn_raw_ptr_buffer_load_lds(r, (LAS void*)l, 16, voff, soff, 0, 0); }
DI void glds16(const void* g, void* l) { __builtin_amdgcn_global_load_lds((const unsigned*)g, (unsigned*)l, 16, 0, 0); }
#define WAIT_V(n) asm volatile("s_waitcnt vmcnt(%0)" ::"n"(n) : "memory")
#define WAIT_L0() asm volatile("s_waitcnt lgkmcnt(0)" ::: "memory")
#define BAR() do { WAIT_L0(); __builtin_amdgcn_s_barrier(); asm volatile("" ::: "memory"); } while (0)
#define SCHED0() __builtin_amdgcn_sched_barrier(0)
#define SGB(m, n) __builtin_amdgcn_sched_group_barrier(m, n, 0)
#define G1_SCHED(NV) do { SGB(0x100, 3); SGB(0x8, 1); SGB(0x100, 2); SGB(0x8, 1); SGB(0x100, 3); SGB(0x8, 1); \
    if ((NV) > 0) SGB(0x10, 1); SGB(0x8, 1); if ((NV) > 1) SGB(0x10, 1); SGB(0x8, 1); if ((NV) > 2) SGB(0x10, 1); SGB(0x8, 1); if ((NV) > 3) SGB(0x10, 1); SCHED0(); } while (0)
#define ST(kid, i) do { __builtin_amdgcn_sched_barrier(0); unsigned long long t_ = __builtin_amdgcn_s_memrealtime(); __builtin_amdgcn_s_waitcnt(0xC07F); if ((threadIdx.x & 63) == 0) ((unsigned long long*)(ws + (64u << 20)))[(((kid) * 256 + blockIdx.x) * 4 + (threadIdx.x >> 6)) * 8 + (i)] = t_; __builtin_amdgcn_sched_barrier(0); } while (0)
DI float xhalf(float v) { return __shfl_xor(v, 32); }
DI bf16x8 pack8(const f32x16& x, int s) {
  u32x4 p;
  p[0] = pk_bf16(x[8 * s + 0], x[8 * s + 1]); p[1] = pk_bf16(x[8 * s + 2], x[8 * s + 3]);
  p[2] = pk_bf16(x[8 * s + 4], x[8 * s + 5]); p[3] = pk_bf16(x[8 * s + 6], x[8 * s + 7]);
  return __builtin_bit_cast(bf16x8, p);
}
DI void dual_softmax(const f32x16& pin, bf16x8* pos, bf16x8* neg) {
  float p[16];
#pragma unroll
  for (int i = 0; i < 8; ++i) { unsigned u = pk_bf16(pin[2 * i], pin[2 * i + 1]); p[2 * i] = bf_lo(u); p[2 * i + 1] = bf_hi(u); }
  float mx = p[0], mn = p[0];
#pragma unroll
  for (int i = 1; i < 16; ++i) { mx = fmaxf(mx, p[i]); mn = fminf(mn, p[i]); }
  mx = fmaxf(mx, xhalf(mx)); mn = fminf(mn, xhalf(mn));
  const float L2E = 1.4426950408889634f;
  float ep[16], en[16], sp = 0.f, sn = 0.f;
  const float mxl = mx * L2E, mnl = mn * L2E;
#pragma unroll
  for (int i = 0; i < 16; ++i) {
    ep[i] = __builtin_amdgcn_exp2f(fmaf(p[i], L2E, -mxl)); en[i] = __builtin_amdgcn_exp2f(fmaf(p[i], -L2E, mnl));
    sp += ep[i]; sn += en[i];
  }
  sp += xhalf(sp); sn += xhalf(sn);
  const float rp = __builtin_amdgcn_rcpf(sp), rn = __builtin_amdgcn_rcpf(sn);
#pragma unroll
  for (int s = 0; s < 2; ++s) {
    u32x4 a, b;
#pragma unroll
    for (int i = 0; i < 4; ++i) {
      a[i] = pk_bf16(ep[8 * s + 2 * i] * rp, ep[8 * s + 2 * i + 1] * rp);
      b[i] = pk_bf16(en[8 * s + 2 * i] * rn, en[8 * s + 2 * i + 1] * rn);
    }
    pos[s] = __builtin_bit_cast(bf16x8, a); neg[s] = __builtin_bit_cast(bf16x8, b);
  }
}

DI void role_a1(int tile, char* sm, const u16* __restrict__ k, const u16* __restrict__ v, const u16* __restrict__ shk, char* __restrict__ ws) {
  const int tid = threadIdx.x, cg = tile & 15, rb = tile >> 4;
  const int cgp = tid & 7, rg = tid >> 3;
  const int ch0 = cg * 64 + cgp * 8, r0 = rb * 128 + rg * 4;
  u32x4 kk[7], vv[4], sk[4];
#pragma unroll
  for (int i = 0; i < 7; ++i) {
    int r = r0 - 3 + i;
    kk[i] = *(const u32x4*)(k + (size_t)(r < 0 ? 0 : r) * D_ + ch0);
    if (r < 0) kk[i] = u32x4{0u, 0u, 0u, 0u};
  }
#pragma unroll
  for (int i = 0; i < 4; ++i) vv[i] = *(const u32x4*)(v + (size_t)(r0 + i) * D_ + ch0);
#pragma unroll
  for (int i = 0; i < 4; ++i) sk[i] = *(const u32x4*)(shk + ch0 * 4 + i * 8);
#pragma unroll
  for (int e = 0; e < 8; ++e) {
    const unsigned w01 = sk[e >> 1][(e & 1) * 2], w23 = sk[e >> 1][(e & 1) * 2 + 1];
    const float w[4] = {bf_lo(w01), bf_hi(w01), bf_lo(w23), bf_hi(w23)};
    float kf[7];
#pragma unroll
    for (int i = 0; i < 7; ++i) kf[i] = (e & 1) ? bf_hi(kk[i][e >> 1]) : bf_lo(kk[i][e >> 1]);
    float o[4];
#pragma unroll
    for (int i = 0; i < 4; ++i) {
      float a = rbf(kf[i] * w[3]);
      a = rbf(a + rbf(kf[i + 1] * w[2]));
      a = rbf(a + rbf(kf[i + 2] * w[1]));
      a = rbf(a + rbf(kf[i + 3] * w[0]));
      const float vf = (e & 1) ? bf_hi(vv[i][e >> 1]) : bf_lo(vv[i][e >> 1]);
      o[i] = a * vf;
    }
    u32x2 w2 = {pk_bf16(o[0], o[1]), pk_bf16(o[2], o[3])};
    *(u32x2*)(sm + (cgp * 8 + e) * 256 + ((rg ^ (2 * cgp)) << 3)) = w2;
  }
  __syncthreads();
#pragma unroll
  for (int it = 0; it < 4; ++it) {
    const int ch = it * 16 + (tid >> 4), l = tid & 15;
    const int c = ((l & 7) << 1) | (l >> 3);
    u32x4 d = *(const u32x4*)(sm + ch * 256 + ((c ^ (ch >> 3)) << 4));
    const int gc = rb * 16 + c;
    *(u32x4*)(ws + WS_XT + (size_t)(cg * 64 + ch) * 4096 + (gc & 1) * 2048 + (gc >> 1) * 16) = d;
  }
}

constexpr int G1_STAGE = 40960, G1_NBUF = 3, G1_WPHI = G1_STAGE * G1_NBUF;
constexpr int G1_LDS = G1_WPHI + 4096;

DI void role_gemm1(int bid, char* sm, const u16* __restrict__ x, const u16* __restrict__ W, const u16* __restrict__ wphi, char* __restrict__ ws) {
  const int tid = threadIdx.x, lane = tid & 63;
  const int wave = __builtin_amdgcn_readfirstlane(tid >> 6);
  const int xcd = bid & 7, idx = bid >> 3;
  const int rb = (xcd & 1) * 8 + (idx & 7), j = (xcd >> 1) * 4 + (idx >> 3);
  const int wm = wave & 1, wn = wave >> 1;
  const int h = lane >> 5, l31 = lane & 31;
  ST(0,0);
  const rsrc_t rx = mk_rsrc(x, 2048 * 1024 * 2), rw = mk_rsrc(W, 1024 * 3072 * 2), rp = mk_rsrc(wphi, 65536);
  unsigned xs[4], wsrc[6];
#pragma unroll
  for (int i = 0; i < 4; ++i) {
    const int p = wave * 4 + i, row = p * 8 + (lane >> 3), ch = (lane & 7) ^ ((row >> 1) & 7);
    xs[i] = ((rb * 128 + row) * 1024 + ch * 8) * 2;
  }
#pragma unroll
  for (int i = 0; i < 6; ++i) {
    const int p = wave * 6 + i, kg = p / 3, seg = p % 3;
    wsrc[i] = ((kg * 8 + ((lane >> 2) & 7)) * 3072 + seg * 1024 + j * 64 + (lane >> 5) * 32 + (lane & 3) * 8) * 2;
  }
  auto piece = [&](int t, int buf, int i) {
    char* b = sm + buf * G1_STAGE;
    if (i < 4) blds16(rx, xs[i], t * 128, b + (wave * 4 + i) * 1024);
    else blds16(rw, wsrc[i - 4], t * (64 * 3072 * 2), b + 16384 + (wave * 6 + i - 4) * 1024);
  };
  blds16(rp, (j * 2048 + wave * 512 + lane * 8) * 2, 0, sm + G1_WPHI + wave * 1024);
#pragma unroll
  for (int i = 0; i < 10; ++i) piece(0, 0, i);
#pragma unroll
  for (int i = 0; i < 10; ++i) piece(1, 1, i);
#pragma unroll
  for (int i = 0; i < 4; ++i) piece(2, 2, i);
  int xoff[4];
  {
    const int row = wm * 64 + l31, sw = (row >> 1) & 7;
#pragma unroll
    for (int s = 0; s < 4; ++s) xoff[s] = row * 128 + (((2 * s + h) ^ sw) << 4);
  }
  const int blk = (lane >> 4) & 1, q4 = (lane & 15) >> 2, p4 = lane & 3;
  const int woff = 16384 + h * 3072 + wn * 512 + q4 * 64 + (2 * blk + (p4 >> 1)) * 16 + (p4 & 1) * 8;
  f32x16 acc[2][3];
#pragma unroll
  for (int a = 0; a < 2; ++a)
#pragma unroll
    for (int b = 0; b < 3; ++b)
#pragma unroll
      for (int i = 0; i < 16; ++i) acc[a][b][i] = 0.f;
  bf16x8 xf[2][2], wf[2][3];
  auto rd = [&](int set, const char* b, int s) {
    xf[set][0] = *(const bf16x8*)(b + xoff[s]);
#pragma unroll
    for (int g = 0; g < 3; ++g) wf[set][g] = cat8(tr_read(b + woff + s * 6144 + g * 1024), tr_read(b + woff + s * 6144 + g * 1024 + 256));
    xf[set][1] = *(const bf16x8*)(b + xoff[s] + 4096);
  };
  auto mm = [&](int set) {
#pragma unroll
    for (int ts = 0; ts < 2; ++ts) {
      acc[ts][0] = mfma32(wf[set][0], xf[set][ts], acc[ts][0]);
      acc[ts][1] = mfma32(wf[set][1], xf[set][ts], acc[ts][1]);
      acc[ts][2] = mfma32(xf[set][ts], wf[set][2], acc[ts][2]);
    }
  };
  ST(0,1);
  WAIT_V(14); BAR();
  ST(0,2);
  rd(0, sm, 0);
  SCHED0();
  int cur = 0, nxt = 1, nx2 = 2;
  auto step = [&](int t, auto BC, auto AA, auto WN, auto RN) {
    const char* b = sm + cur * G1_STAGE;
    rd(1, b, 1);
    if constexpr (BC.value) { piece(t + 2, nx2, 4); piece(t + 2, nx2, 5); piece(t + 2, nx2, 6); }
    mm(0);
    G1_SCHED(BC.value ? 3 : 0);
    rd(0, b, 2);
    if constexpr (BC.value) { piece(t + 2, nx2, 7); piece(t + 2, nx2, 8); piece(t + 2, nx2, 9); }
    mm(1);
    G1_SCHED(BC.value ? 3 : 0);
    rd(1, b, 3); mm(0);
    G1_SCHED(0);
    WAIT_V(WN.value);
    BAR();
    SCHED0();
    if constexpr (RN.value) rd(0, sm + nxt * G1_STAGE, 0);
    if constexpr (AA.value) { piece(t + 3, cur, 0); piece(t + 3, cur, 1); piece(t + 3, cur, 2); piece(t + 3, cur, 3); }
    mm(1);
    G1_SCHED(AA.value ? 4 : 0);
    const int o = cur; cur = nxt; nxt = nx2; nx2 = o;
  };
  using T_ = std::true_type; using F_ = std::false_type;
#pragma unroll 1
  for (int t = 0; t < 13; ++t) step(t, T_{}, T_{}, std::integral_constant<int, 10>{}, T_{});
  step(13, T_{}, F_{}, std::integral_constant<int, 10>{}, T_{});
  step(14, F_{}, F_{}, std::integral_constant<int, 0>{}, T_{});
  step(15, F_{}, F_{}, std::integral_constant<int, 0>{}, F_{});
  ST(0,3);
  const int head = 2 * j + wn;
#pragma unroll
  for (int ts = 0; ts < 2; ++ts) {
    char* dst = ws + WS_QF + ((size_t)((rb * 4 + wm * 2 + ts) * 32 + head) * 2) * 1024 + lane * 16;
    *(bf16x8*)dst = pack8(acc[ts][0], 0);
    *(bf16x8*)(dst + 1024) = pack8(acc[ts][0], 1);
  }
  bf16x8 wp[2];
  {
    const char* wb = sm + G1_WPHI + wn * 2048 + (4 * h + q4) * 64 + (2 * blk + (p4 >> 1)) * 16 + (p4 & 1) * 8;
#pragma unroll
    for (int s = 0; s < 2; ++s) wp[s] = cat8(tr_read(wb + s * 1024), tr_read(wb + s * 1024 + 512));
  }
  char* img = sm + wave * 8192;
  f32x16 zero;
#pragma unroll
  for (int i = 0; i < 16; ++i) zero[i] = 0.f;
#pragma unroll
  for (int ts = 0; ts < 2; ++ts) {
    f32x16 pk = mfma32(wp[0], pack8(acc[ts][1], 0), zero);
    pk = mfma32(wp[1], pack8(acc[ts][1], 1), pk);
    bf16x8 pos[2], neg[2];
    dual_softmax(pk, pos, neg);
    const int row = ts * 32 + l31, sw = (row >> 1) & 7;
#pragma unroll
    for (int g = 0; g < 4; ++g) {
      const u32x4 pp = __builtin_bit_cast(u32x4, pos[g >> 1]), nn = __builtin_bit_cast(u32x4, neg[g >> 1]);
      u32x2 a = {pp[(g & 1) * 2], pp[(g & 1) * 2 + 1]}, b = {nn[(g & 1) * 2], nn[(g & 1) * 2 + 1]};
      const int o = row * 64 + (((2 * g + h) ^ sw) << 3);
      *(u32x2*)(img + o) = a;
      *(u32x2*)(img + 4096 + o) = b;
    }
  }
  ST(0,4);
  f32x16 kvacc[2], zacc[2];
  kvacc[0] = zero; kvacc[1] = zero; zacc[0] = zero; zacc[1] = zero;
  bf16x8 ones;
#pragma unroll
  for (int i = 0; i < 8; ++i) ones[i] = (short)0x3f80;
#pragma unroll
  for (int ts = 0; ts < 2; ++ts)
#pragma unroll
    for (int s = 0; s < 2; ++s) {
      const bf16x8 vb = pack8(acc[ts][2], s);
      const int r0 = ts * 32 + 16 * s + 4 * h + q4, r1 = r0 + 8;
      const int o0 = r0 * 64 + (((4 * blk + p4) ^ ((r0 >> 1) & 7)) << 3), o1 = r1 * 64 + (((4 * blk + p4) ^ ((r1 >> 1) & 7)) << 3);
#pragma unroll
      for (int tl = 0; tl < 2; ++tl) {
        const bf16x8 pa = cat8(tr_read(img + tl * 4096 + o0), tr_read(img + tl * 4096 + o1));
        kvacc[tl] = mfma32(pa, vb, kvacc[tl]);
        zacc[tl] = mfma32(pa, ones, zacc[tl]);
      }
    }
  ST(0,5);
  char* red = sm + 32768 + wn * 8704;
  if (wm == 1) {
#pragma unroll
    for (int tl = 0; tl < 2; ++tl)
#pragma unroll
      for (int qd = 0; qd < 4; ++qd) {
        f32x4 t = {kvacc[tl][4 * qd], kvacc[tl][4 * qd + 1], kvacc[tl][4 * qd + 2], kvacc[tl][4 * qd + 3]};
        *(f32x4*)(red + (tl * 4 + qd) * 1024 + lane * 16) = t;
        if (l31 == 0) {
          f32x4 z = {zacc[tl][4 * qd], zacc[tl][4 * qd + 1], zacc[tl][4 * qd + 2], zacc[tl][4 * qd + 3]};
          *(f32x4*)(red + 8192 + ((tl * 2 + h) * 4 + qd) * 16) = z;
        }
      }
  }
  BAR();
  if (wm == 0) {
    char* dst = ws + WS_KVP + (size_t)(rb * 32 + head) * 8192 + lane * 16;
    char* zdst = ws + WS_ZP + (size_t)(rb * 32 + head) * 256;
#pragma unroll
    for (int tl = 0; tl < 2; ++tl)
#pragma unroll
      for (int qd = 0; qd < 4; ++qd) {
        f32x4 t = *(const f32x4*)(red + (tl * 4 + qd) * 1024 + lane * 16);
        t[0] += kvacc[tl][4 * qd]; t[1] += kvacc[tl][4 * qd + 1]; t[2] += kvacc[tl][4 * qd + 2]; t[3] += kvacc[tl][4 * qd + 3];
        *(f32x4*)(dst + (tl * 4 + qd) * 1024) = t;
        if (l31 == 0) {
          f32x4 z = *(const f32x4*)(red + 8192 + ((tl * 2 + h) * 4 + qd) * 16);
          z[0] += zacc[tl][4 * qd]; z[1] += zacc[tl][4 * qd + 1]; z[2] += zacc[tl][4 * qd + 2]; z[3] += zacc[tl][4 * qd + 3];
          *(f32x4*)(zdst + ((tl * 2 + h) * 4 + qd) * 16) = z;
        }
      }
  }
  ST(0,6);
}

__global__ __launch_bounds__(256, 2) void k1_gemm(const u16* x, const u16* W, const u16* wphi, char* ws) {
  extern __shared__ __attribute__((aligned(16))) char sm[];
  role_gemm1(blockIdx.x, sm, x, W, wphi, ws);
}
__global__ __launch_bounds__(256) void k1_a1(const u16* k, const u16* v, const u16* shk, char* ws) {
  __shared__ __attribute__((aligned(16))) char sm[16384];
  role_a1(blockIdx.x, sm, k, v, shk, ws);
}

constexpr int CV_WIN = 16512 + 512 + 64, CV_XR = 144 * 16, CV_WAVE = CV_WIN + 2 * CV_XR;
constexpr int CV_LDS = 4 * CV_WAVE;

DI int win_addr(int m0) {
  const int u = m0 + 16;
  return ((u >> 3) << 6) + ((u >> 8) << 6) + ((((u >> 1) & 3) ^ ((u >> 4) & 3)) << 4) + ((u & 1) << 3);
}

DI void role_conv(int bid, char* sm, const u16* __restrict__ hk, const u16* __restrict__ Dv, char* __restrict__ ws) {
  const int tid = threadIdx.x, lane = tid & 63;
  const int wave = __builtin_amdgcn_readfirstlane(tid >> 6);
  const int ch = bid * 4 + wave;
  char* wl = sm + wave * CV_WAVE;
  char* xl = wl + CV_WIN;
  const u16* hrow = hk + (size_t)ch * 2048;
  const char* xsrc = ws + WS_XT + (size_t)ch * 4096;
  ST(1,0);
  u32x4 hv[4], xv[4]; u32x2 hp[4];
#pragma unroll
  for (int b = 0; b < 4; ++b) {
    const int e0 = b * 512 + lane * 8;
    hv[b] = *(const u32x4*)(hrow + e0);
    hp[b] = *(const u32x2*)(hrow + (e0 > 0 ? e0 - 4 : 0));
    xv[b] = *(const u32x4*)(xsrc + b * 1024 + lane * 16);
  }
  const float dcoef = bf_lo((unsigned)Dv[ch]);
  if (lane == 0) { hp[0][0] = 0u; hp[0][1] = 0u; }
  {
    u32x4 z = {0u, 0u, 0u, 0u};
    if (lane < 8) *(u32x4*)(wl + lane * 16) = z;
    if (lane < 32) *(u32x4*)(xl + (lane >> 4) * CV_XR + (lane & 15) * 16) = z;
  }
#pragma unroll
  for (int b = 0; b < 4; ++b) {
    *(u32x4*)(xl + (b >> 1) * CV_XR + 256 + ((b & 1) * 64 + lane) * 16) = xv[b];
    const unsigned dm2 = hp[b][0], dm1 = hp[b][1], d0 = hv[b][0], d1 = hv[b][1], d2 = hv[b][2], d3 = hv[b][3];
    unsigned R[10];
    R[0] = __builtin_amdgcn_perm(dm2, dm1, 0x07060100u);
    R[1] = __builtin_amdgcn_alignbit(dm1, dm1, 16);
    R[2] = __builtin_amdgcn_perm(dm1, d0, 0x07060100u);
    R[3] = __builtin_amdgcn_alignbit(d0, d0, 16);
    R[4] = __builtin_amdgcn_perm(d0, d1, 0x07060100u);
    R[5] = __builtin_amdgcn_alignbit(d1, d1, 16);
    R[6] = __builtin_amdgcn_perm(d1, d2, 0x07060100u);
    R[7] = __builtin_amdgcn_alignbit(d2, d2, 16);
    R[8] = __builtin_amdgcn_perm(d2, d3, 0x07060100u);
    R[9] = __builtin_amdgcn_alignbit(d3, d3, 16);
    const int i = b * 64 + lane + 2;
    const int sw = (i >> 1) & 3;
#pragma unroll
    for (int r = 0; r < 4; ++r) {
      u32x4 w = {R[2 * r + 2], R[2 * r], R[2 * r + 3], R[2 * r + 1]};
      *(u32x4*)(wl + i * 64 + ((i >> 5) << 6) + ((r ^ sw) << 4)) = w;
    }
  }
  ST(1,1);
  const int a = lane & 15, g = lane >> 4;
  const int off = (g & 1) * 16 + (g >> 1) * 8;
  const int cb = 2 * a + 1 - (off >> 3);
  const int xb0 = CV_WIN + (cb & 1) * CV_XR + (((cb >> 1) + 16) << 4);
  f32x4 acc[8];
#pragma unroll
  for (int c = 0; c < 8; ++c) acc[c] = f32x4{0.f, 0.f, 0.f, 0.f};
  bf16x8 A[2][8], B[2][8];
  auto ld = [&](int set, int t0) {
    const int m0 = 32 * t0 + a + off - 8;
    const char* pa0 = wl + win_addr(m0);
    const char* pa1 = wl + win_addr(m0 - 4);
    const char* pb = wl + xb0 - t0 * 32;
#pragma unroll
    for (int i = 0; i < 8; ++i) {
      A[set][i] = cat8(*(const s16x4*)(pa0 + i * 2112), *(const s16x4*)(pa1 + i * 2112));
      B[set][i] = *(const bf16x8*)(pb + i * 256);
    }
  };
  auto mm = [&](int set) {
#pragma unroll
    for (int t1 = 0; t1 < 8; ++t1)
#pragma unroll
      for (int i = 0; i + t1 < 8; ++i) acc[t1 + i] = mfma16(A[set][t1], B[set][i], acc[t1 + i]);
  };
  ld(0, 0);
  SCHED0();
#define CV_SCHED() do { _Pragma("unroll") for (int z = 0; z < 24; ++z) { SGB(0x8, 1); SGB(0x100, 1); } SGB(0x8, 12); SCHED0(); } while (0)
#pragma unroll 1
  for (int t0 = 0; t0 < 6; t0 += 2) {
    ld(1, t0 + 1); mm(0); CV_SCHED();
    ld(0, t0 + 2); mm(1); CV_SCHED();
  }
  ld(1, 7); mm(0); CV_SCHED();
  mm(1);
  ST(1,2);
  char* ydst = ws + WS_YT + (size_t)ch * 4096 + (16 * a + 4 * g) * 2;
  const int xe = CV_WIN + (g >> 1) * CV_XR + ((a + 16) << 4) + (g & 1) * 8;
#pragma unroll
  for (int c = 0; c < 8; ++c) {
    const u32x2 kv = *(const u32x2*)(wl + xe + c * 256);
    const unsigned y01 = pk_bf16(acc[c][0], acc[c][1]), y23 = pk_bf16(acc[c][2], acc[c][3]);
    const unsigned t01 = pk_bf16(bf_lo(kv[0]) * dcoef, bf_hi(kv[0]) * dcoef), t23 = pk_bf16(bf_lo(kv[1]) * dcoef, bf_hi(kv[1]) * dcoef);
    u32x2 o = {pk_bf16(bf_lo(y01) + bf_lo(t01), bf_hi(y01) + bf_hi(t01)), pk_bf16(bf_lo(y23) + bf_lo(t23), bf_hi(y23) + bf_hi(t23))};
    *(u32x2*)(ydst + c * 512) = o;
  }
  ST(1,3);
}

DI void role_o(int bid, char* sm, const u16* __restrict__ wphi, char* __restrict__ ws) {
  const int tid = threadIdx.x, lane = tid & 63, wave = tid >> 6;
  const int head = (bid & 7) * 4 + ((bid >> 3) & 3), tb = bid >> 5;
  const int h = lane >> 5;
  const int blk = (lane >> 4) & 1, q4 = (lane & 15) >> 2, p4 = lane & 3;
  bf16x8 qf[2][2];
#pragma unroll
  for (int ts = 0; ts < 2; ++ts) {
    const char* src = ws + WS_QF + ((size_t)((tb * 8 + wave * 2 + ts) * 32 + head) * 2) * 1024 + lane * 16;
    qf[ts][0] = *(const bf16x8*)src; qf[ts][1] = *(const bf16x8*)(src + 1024);
  }
  if (tid < 128) *(u32x4*)(sm + 8192 + tid * 16) = *(const u32x4*)(wphi + head * 1024 + tid * 8);
  {
    const char* src = ws + WS_KVP + (size_t)head * 8192 + tid * 16;
    f32x4 s0 = {0.f, 0.f, 0.f, 0.f}, s1 = s0, e0[16], e1[16];
#pragma unroll
    for (int r = 0; r < 16; ++r) { e0[r] = *(const f32x4*)(src + (size_t)r * 32 * 8192); e1[r] = *(const f32x4*)(src + (size_t)r * 32 * 8192 + 4096); }
    float zs = 0.f, ze[16];
    if (tid < 64) {
#pragma unroll
      for (int r = 0; r < 16; ++r) ze[r] = *(const float*)(ws + WS_ZP + (size_t)(r * 32 + head) * 256 + tid * 4);
#pragma unroll
      for (int r = 0; r < 16; ++r) zs += ze[r];
    }
#pragma unroll
    for (int r = 0; r < 16; ++r) { s0 += e0[r]; s1 += e1[r]; }
    const int qd = tid >> 6;
    u32x2 a = {pk_bf16(s0[0], s0[1]), pk_bf16(s0[2], s0[3])}, b = {pk_bf16(s1[0], s1[1]), pk_bf16(s1[2], s1[3])};
    *(u32x2*)(sm + (qd >> 1) * 1024 + lane * 16 + (qd & 1) * 8) = a;
    *(u32x2*)(sm + 2048 + (qd >> 1) * 1024 + lane * 16 + (qd & 1) * 8) = b;
    if (tid < 64) *(u16*)(sm + 4096 + tid * 2) = (u16)(pk_bf16(zs, 0.f) & 0xffffu);
  }
  __syncthreads();
  bf16x8 kvf[4], zf[4], wp[2];
#pragma unroll
  for (int i = 0; i < 4; ++i) {
    kvf[i] = *(const bf16x8*)(sm + i * 1024 + lane * 16);
    zf[i] = *(const bf16x8*)(sm + 4096 + ((i >> 1) * 2 + h) * 32 + (i & 1) * 16);
  }
  {
    const char* wb = sm + 8192 + (4 * h + q4) * 64 + (2 * blk + (p4 >> 1)) * 16 + (p4 & 1) * 8;
#pragma unroll
    for (int s = 0; s < 2; ++s) wp[s] = cat8(tr_read(wb + s * 1024), tr_read(wb + s * 1024 + 512));
  }
  f32x16 zero;
#pragma unroll
  for (int i = 0; i < 16; ++i) zero[i] = 0.f;
#pragma unroll
  for (int ts = 0; ts < 2; ++ts) {
    f32x16 pq = mfma32(wp[0], qf[ts][0], zero);
    pq = mfma32(wp[1], qf[ts][1], pq);
    bf16x8 ph[4];
    dual_softmax(pq, ph, ph + 2);
    f32x16 num = zero, den = zero;
#pragma unroll
    for (int i = 0; i < 4; ++i) { num = mfma32(kvf[i], ph[i], num); den = mfma32(zf[i], ph[i], den); }
    const float dn = rbf(rbf(den[0]) + 1e-6f);
    const float rd = 1.0f / dn;
    f32x16 o;
#pragma unroll
    for (int i = 0; i < 8; ++i) {
      const unsigned u = pk_bf16(num[2 * i], num[2 * i + 1]);
      o[2 * i] = bf_lo(u) * rd; o[2 * i + 1] = bf_hi(u) * rd;
    }
    char* dst = ws + WS_OF + ((size_t)((tb * 8 + wave * 2 + ts) * 32 + head) * 2) * 1024 + lane * 16;
    *(bf16x8*)dst = pack8(o, 0);
    *(bf16x8*)(dst + 1024) = pack8(o, 1);
  }
}

__global__ __launch_bounds__(256, 2) void k2_conv(const u16* hk, const u16* Dv, char* ws) {
  extern __shared__ __attribute__((aligned(16))) char sm[];
  role_conv(blockIdx.x, sm, hk, Dv, ws);
}
__global__ __launch_bounds__(256, 2) void k2_o(const u16* wphi, char* ws) {
  __shared__ __attribute__((aligned(16))) char sm[10240];
  role_o(blockIdx.x, sm, wphi, ws);
}

DI void role_a3(int tile, char* sm, const u16* __restrict__ q, const char* __restrict__ ws, u16* __restrict__ out) {
  const int tid = threadIdx.x, cg = tile & 15, rb = tile >> 4;
  const int cgp = tid & 7, rg = tid >> 3;
  const int ch0 = cg * 64 + cgp * 8, r0 = rb * 128 + rg * 4;
  u32x4 qq[4], yy[4];
#pragma unroll
  for (int it = 0; it < 4; ++it) {
    const int ch = it * 16 + (tid >> 4), c = tid & 15;
    yy[it] = *(const u32x4*)(ws + WS_YT + (size_t)(cg * 64 + ch) * 4096 + rb * 256 + c * 16);
  }
#pragma unroll
  for (int i = 0; i < 4; ++i) qq[i] = *(const u32x4*)(q + (size_t)(r0 + i) * D_ + ch0);
#pragma unroll
  for (int it = 0; it < 4; ++it) {
    const int ch = it * 16 + (tid >> 4), c = tid & 15;
    *(u32x4*)(sm + ch * 256 + ((c ^ (ch >> 3)) << 4)) = yy[it];
  }
  __syncthreads();
  u32x2 y[8];
#pragma unroll
  for (int e = 0; e < 8; ++e) y[e] = *(const u32x2*)(sm + (cgp * 8 + e) * 256 + ((rg ^ (2 * cgp)) << 3));
#pragma unroll
  for (int i = 0; i < 4; ++i) {
    u32x4 o;
#pragma unroll
    for (int e2 = 0; e2 < 4; ++e2) {
      const unsigned ya = y[2 * e2][i >> 1], yb = y[2 * e2 + 1][i >> 1];
      const float fa = (i & 1) ? bf_hi(ya) : bf_lo(ya), fb = (i & 1) ? bf_hi(yb) : bf_lo(yb);
      o[e2] = pk_bf16(bf_lo(qq[i][e2]) * fa, bf_hi(qq[i][e2]) * fb);
    }
    *(u32x4*)(out + (size_t)(r0 + i) * 2048 + ch0) = o;
  }
}

constexpr int G2_STAGE = 24576, G2_NBUF = 4, G2_LDS = G2_STAGE * G2_NBUF;

DI void role_gemm2(int bid, char* sm, const u16* __restrict__ wo, const char* __restrict__ ws, u16* __restrict__ out) {
  const int tid = threadIdx.x, lane = tid & 63;
  const int wave = __builtin_amdgcn_readfirstlane(tid >> 6);
  const int xcd = bid & 7, idx = bid >> 3;
  const int rb = (xcd & 1) * 8 + (idx & 7), nb = (xcd >> 1) * 4 + (idx >> 3);
  const int h = lane >> 5, l31 = lane & 31;
  ST(2,0);
  const rsrc_t ro = mk_rsrc(ws + WS_OF, 4u << 20), rw = mk_rsrc(wo, 2u << 20);
  const unsigned osrc = (rb * 4 + wave) * 65536 + lane * 16;
  const unsigned wsrc = ((wave * 16 + ((lane >> 2) & 7)) * 1024 + nb * 64 + (lane >> 5) * 32 + (lane & 3) * 8) * 2;
  auto piece = [&](int t, int buf, int i) {
    char* b = sm + buf * G2_STAGE;
    if (i < 4) blds16(ro, osrc + i * 1024, t * 4096, b + wave * 4096 + i * 1024);
    else blds16(rw, wsrc + (i - 4) * 16384, t * 131072, b + 16384 + (wave * 2 + i - 4) * 1024);
  };
#pragma unroll
  for (int t = 0; t < 3; ++t)
#pragma unroll
    for (int i = 0; i < 6; ++i) piece(t, t, i);
  piece(3, 3, 0); piece(3, 3, 1);
  const int blk = (lane >> 4) & 1, q4 = (lane & 15) >> 2, p4 = lane & 3;
  const int woff = 16384 + (4 * h + q4) * 64 + (2 * blk + (p4 >> 1)) * 16 + (p4 & 1) * 8;
  const int ooff = wave * 4096 + lane * 16;
  f32x16 acc[2];
#pragma unroll
  for (int b = 0; b < 2; ++b)
#pragma unroll
    for (int i = 0; i < 16; ++i) acc[b][i] = 0.f;
  bf16x8 of[2], wf[2][2];
  auto rd = [&](int set, const char* b, int s) {
    of[set] = *(const bf16x8*)(b + ooff + s * 1024);
#pragma unroll
    for (int g = 0; g < 2; ++g) wf[set][g] = cat8(tr_read(b + woff + s * 2048 + g * 512), tr_read(b + woff + s * 2048 + 1024 + g * 512));
  };
  auto mm = [&](int set) {
    acc[0] = mfma32(wf[set][0], of[set], acc[0]);
    acc[1] = mfma32(wf[set][1], of[set], acc[1]);
  };
  ST(2,1);
  WAIT_V(14); BAR();
  ST(2,2);
  rd(0, sm, 0);
  SCHED0();
  int cur = 0;
#define G2_SCHED(NV) do { SGB(0x100, 3); SGB(0x8, 1); SGB(0x100, 2); if ((NV) > 0) SGB(0x10, 1); SGB(0x8, 1); if ((NV) > 1) SGB(0x10, 1); SCHED0(); } while (0)
  auto step = [&](int t, auto BC, auto AA, auto WN, auto RN) {
    const char* b = sm + cur * G2_STAGE;
    const int nxt = (cur + 1) & 3, prv = (cur + 3) & 3;
    rd(1, b, 1);
    if constexpr (BC.value) { piece(t + 3, prv, 2); piece(t + 3, prv, 3); }
    mm(0); G2_SCHED(BC.value ? 2 : 0);
    rd(0, b, 2);
    if constexpr (BC.value) { piece(t + 3, prv, 4); piece(t + 3, prv, 5); }
    mm(1); G2_SCHED(BC.value ? 2 : 0);
    rd(1, b, 3); mm(0); G2_SCHED(0);
    WAIT_V(WN.value);
    BAR();
    SCHED0();
    if constexpr (RN.value) rd(0, sm + nxt * G2_STAGE, 0);
    if constexpr (AA.value) { piece(t + 4, cur, 0); piece(t + 4, cur, 1); }
    mm(1); G2_SCHED(AA.value ? 2 : 0);
    cur = nxt;
  };
  using T_ = std::true_type; using F_ = std::false_type;
#pragma unroll 1
  for (int t = 0; t < 12; ++t) step(t, T_{}, T_{}, std::integral_constant<int, 12>{}, T_{});
  step(12, T_{}, F_{}, std::integral_constant<int, 12>{}, T_{});
  step(13, F_{}, F_{}, std::integral_constant<int, 6>{}, T_{});
  step(14, F_{}, F_{}, std::integral_constant<int, 0>{}, T_{});
  step(15, F_{}, F_{}, std::integral_constant<int, 0>{}, F_{});
  ST(2,3);
  char* ot = sm + wave * 4096;
#pragma unroll
  for (int g = 0; g < 2; ++g)
#pragma unroll
    for (int qd = 0; qd < 4; ++qd) {
      u32x2 w = {pk_bf16(acc[g][4 * qd], acc[g][4 * qd + 1]), pk_bf16(acc[g][4 * qd + 2], acc[g][4 * qd + 3])};
      *(u32x2*)(ot + l31 * 128 + (((4 * g + qd) ^ (l31 & 7)) << 4) + h * 8) = w;
    }
  WAIT_L0();
#pragma unroll
  for (int it = 0; it < 4; ++it) {
    const int row = it * 8 + (lane >> 3), c = lane & 7;
    const u32x4 d = *(const u32x4*)(ot + row * 128 + ((c ^ (row & 7)) << 4));
    *(u32x4*)(out + (size_t)(rb * 128 + wave * 32 + row) * 2048 + 1024 + nb * 64 + c * 8) = d;
  }
  ST(2,4);
}

__global__ __launch_bounds__(256, 2) void k3_gemm(const u16* wo, const char* ws, u16* out) {
  extern __shared__ __attribute__((aligned(16))) char sm[];
  role_gemm2(blockIdx.x, sm, wo, ws, out);
}
__global__ __launch_bounds__(256) void k3_a3(const u16* q, const char* ws, u16* out) {
  __shared__ __attribute__((aligned(16))) char sm[16384];
  role_a3(blockIdx.x, sm, q, ws, out);
}

__global__ void k_spin(char* ws) {
  const unsigned long long* st = (const unsigned long long*)(ws + (64u << 20));
  unsigned long long amn = ~0ull, amx = 0, bmn = ~0ull, bmx = 0;
  for (int i = threadIdx.x; i < 1024; i += 64) {
    unsigned long long a = st[(KA * 1024 + i) * 8 + IA], b = st[(KB * 1024 + i) * 8 + IB];
    amn = a < amn ? a : amn; amx = a > amx ? a : amx; bmn = b < bmn ? b : bmn; bmx = b > bmx ? b : bmx;
  }
  for (int o = 32; o; o >>= 1) {
    unsigned long long t;
    t = __shfl_xor(amn, o); amn = t < amn ? t : amn; t = __shfl_xor(amx, o); amx = t > amx ? t : amx;
    t = __shfl_xor(bmn, o); bmn = t < bmn ? t : bmn; t = __shfl_xor(bmx, o); bmx = t > bmx ? t : bmx;
  }
  long long T = (long long)((BMAX ? bmx : bmn) - (AMIN ? amn : amx));
  if (T < 0) T = 0;
  if (T > 10000) T = 10000;
  unsigned long long r0 = __builtin_amdgcn_s_memrealtime();
  while (__builtin_amdgcn_s_memrealtime() - r0 < (unsigned long long)T * 10) __builtin_amdgcn_s_sleep(1);
}
extern "C" void kernel_launch(void* const* d_in, const int* in_sizes, int n_in, void* d_out, int out_size, void* d_ws, size_t ws_size,
                              hipStream_t stream) {
  const u16 *q = (const u16*)d_in[0], *k = (const u16*)d_in[1], *v = (const u16*)d_in[2], *shk = (const u16*)d_in[3];
  const u16 *hk = (const u16*)d_in[4], *Dv = (const u16*)d_in[5], *x = (const u16*)d_in[6], *W = (const u16*)d_in[7];
  const u16 *wphi = (const u16*)d_in[8], *wo = (const u16*)d_in[9];
  u16* out = (u16*)d_out; char* ws = (char*)d_ws;
  hipFuncSetAttribute((const void*)k1_gemm, hipFuncAttributeMaxDynamicSharedMemorySize, G1_LDS);
  hipFuncSetAttribute((const void*)k2_conv, hipFuncAttributeMaxDynamicSharedMemorySize, CV_LDS);
  hipFuncSetAttribute((const void*)k3_gemm, hipFuncAttributeMaxDynamicSharedMemorySize, G2_LDS);
  k1_a1<<<256, 256, 0, stream>>>(k, v, shk, ws);
  k1_gemm<<<256, 256, G1_LDS, stream>>>(x, W, wphi, ws);
  k2_conv<<<256, 256, CV_LDS, stream>>>(hk, Dv, ws);
  k2_o<<<256, 256, 0, stream>>>(wphi, ws);
  k3_gemm<<<256, 256, G2_LDS, stream>>>(wo, ws, out);
  k3_a3<<<256, 256, 0, stream>>>(q, ws, out);
  k_spin<<<1, 64, 0, stream>>>(ws);
}
```

```cpp
#include <hip/hip_runtime.h>
#include <stdint.h>
#include <type_traits>

typedef short bf16x8 __attribute__((ext_vector_type(8)));
typedef short s16x4 __attribute__((ext_vector_type(4)));
typedef float f32x4 __attribute__((ext_vector_type(4)));
typedef float f32x2 __attribute__((ext_vector_type(2)));
typedef float f32x16 __attribute__((ext_vector_type(16)));
typedef unsigned u32x4 __attribute__((ext_vector_type(4)));
typedef unsigned u32x2 __attribute__((ext_vector_type(2)));
typedef __bf16 bfx2 __attribute__((ext_vector_type(2)));
typedef unsigned short u16;
typedef unsigned long long u64;
#define DI __device__ __forceinline__
#define LAS __attribute__((address_space(3)))
#define GAS __attribute__((address_space(1)))
typedef __amdgpu_buffer_rsrc_t rsrc_t;

constexpr unsigned WS_XT = 0;
constexpr unsigned WS_YT = 4u << 20;
constexpr unsigned WS_OF = 8u << 20;
constexpr unsigned WS_KVP = 12u << 20;
constexpr unsigned WS_ZP = 16u << 20;
constexpr unsigned WS_KVF = 17u << 20;
constexpr unsigned WS_ZF = WS_KVF + 131072;
constexpr unsigned WS_FL = 18u << 20;
constexpr unsigned FL_A1 = WS_FL, FL_Y = WS_FL + 8192, FL_O = WS_FL + 16384, FL_P = WS_FL + 24576, FL_R = WS_FL + 28672, FL_EPOCH = WS_FL + 32768;
constexpr unsigned WS_USED = 19u << 20;
constexpr int LDS_BYTES = 163840;

DI unsigned pk_bf16(float lo, float hi) { f32x2 p = {lo, hi}; return __builtin_bit_cast(unsigned, __builtin_convertvector(p, bfx2)); }
DI float bf_lo(unsigned u) { return __uint_as_float(u << 16); }
DI float bf_hi(unsigned u) { return __uint_as_float(u & 0xffff0000u); }
DI float rbf(float x) { return bf_lo(pk_bf16(x, 0.f)); }
DI f32x16 mfma32(bf16x8 a, bf16x8 b, f32x16 c) { return __builtin_amdgcn_mfma_f32_32x32x16_bf16(a, b, c, 0, 0, 0); }
DI f32x4 mfma16(bf16x8 a, bf16x8 b, f32x4 c) { return __builtin_amdgcn_mfma_f32_16x16x32_bf16(a, b, c, 0, 0, 0); }
DI s16x4 tr_read(const char* p) { return __builtin_amdgcn_ds_read_tr16_b64_v4i16((LAS s16x4*)p); }
DI bf16x8 cat8(s16x4 a, s16x4 b) { return __builtin_shufflevector(a, b, 0, 1, 2, 3, 4, 5, 6, 7); }
DI rsrc_t mk_rsrc(const void* p, unsigned bytes) { return __builtin_amdgcn_make_buffer_rsrc((void*)p, 0, bytes, 0x00020000); }
DI void blds16(rsrc_t r, unsigned voff, unsigned soff, void* l) { __builtin_amdgcn_raw_ptr_buffer_load_lds(r, (LAS void*)l, 16, voff, soff, 0, 0); }
#define LDAUX 16
#define STAUX 16
DI u32x4 ld16_sc1(rsrc_t r, unsigned voff, unsigned soff) { return __builtin_amdgcn_raw_buffer_load_b128(r, voff, soff, LDAUX); }
DI void st16_sc1(u32x4 v, rsrc_t r, unsigned voff, unsigned soff) { __builtin_amdgcn_raw_buffer_store_b128(v, r, voff + soff, 0, STAUX); }
#define WAIT_V(n) asm volatile("s_waitcnt vmcnt(%0)" ::"n"(n) : "memory")
#define WAIT_L0() asm volatile("s_waitcnt lgkmcnt(0)" ::: "memory")
#define BAR() do { WAIT_L0(); __builtin_amdgcn_s_barrier(); asm volatile("" ::: "memory"); } while (0)
#define SCHED0() __builtin_amdgcn_sched_barrier(0)
#define SGB(m, n) __builtin_amdgcn_sched_group_barrier(m, n, 0)
#define ST(kid, i) SCHED0()
template <int N> using IC = std::integral_constant<int, N>;
using T_ = std::true_type; using F_ = std::false_type;

DI float xhalf(float v) { return __shfl_xor(v, 32); }
DI bf16x8 pack8(const f32x16& x, int s) {
  u32x4 p;
  p[0] = pk_bf16(x[8 * s + 0], x[8 * s + 1]); p[1] = pk_bf16(x[8 * s + 2], x[8 * s + 3]);
  p[2] = pk_bf16(x[8 * s + 4], x[8 * s + 5]); p[3] = pk_bf16(x[8 * s + 6], x[8 * s + 7]);
  return __builtin_bit_cast(bf16x8, p);
}
DI u64 flag_value(unsigned epoch) { const unsigned t = epoch + 1u; return ((u64)t << 32) | (u64)(t ^ 0x5bd1e995u); }
DI unsigned load_epoch(const char* ws) {
  return __builtin_amdgcn_readfirstlane(__hip_atomic_load((GAS const unsigned*)(ws + FL_EPOCH), __ATOMIC_RELAXED, __HIP_MEMORY_SCOPE_AGENT));
}
template <int YOUNGER = 0>
DI void publish(char* ws, unsigned flag_off, u64 fv, int lane) {
  WAIT_V(YOUNGER);
  if (lane == 0) __hip_atomic_store((GAS u64*)(ws + flag_off), fv, __ATOMIC_RELAXED, __HIP_MEMORY_SCOPE_AGENT);
}
DI void poll(const char* ws, unsigned flag_off, int n, u64 fv, int lane) {
  const GAS u64* p = (const GAS u64*)(ws + flag_off) + (lane < n ? lane : 0);
  for (unsigned spins = 0; spins < (1u << 20); ++spins) {
    const u64 v = __hip_atomic_load(p, __ATOMIC_RELAXED, __HIP_MEMORY_SCOPE_AGENT);
    if (__all(v == fv)) break;
#define PSLEEP 2
    __builtin_amdgcn_s_sleep(PSLEEP);
  }
  __builtin_amdgcn_fence(__ATOMIC_ACQUIRE, "wavefront");
  asm volatile("" ::: "memory");
}

DI void dual_softmax(const f32x16& pin, bf16x8* pos, bf16x8* neg) {
  float p[16];
#pragma unroll
  for (int i = 0; i < 8; ++i) { unsigned u = pk_bf16(pin[2 * i], pin[2 * i + 1]); p[2 * i] = bf_lo(u); p[2 * i + 1] = bf_hi(u); }
  float mx = p[0], mn = p[0];
#pragma unroll
  for (int i = 1; i < 16; ++i) { mx = fmaxf(mx, p[i]); mn = fminf(mn, p[i]); }
  mx = fmaxf(mx, xhalf(mx)); mn = fminf(mn, xhalf(mn));
  const float L2E = 1.4426950408889634f;
  float ep[16], en[16], sp = 0.f, sn = 0.f;
  const float mxl = mx * L2E, mnl = mn * L2E;
#pragma unroll
  for (int i = 0; i < 16; ++i) {
    ep[i] = __builtin_amdgcn_exp2f(fmaf(p[i], L2E, -mxl)); en[i] = __builtin_amdgcn_exp2f(fmaf(p[i], -L2E, mnl));
    sp += ep[i]; sn += en[i];
  }
  sp += xhalf(sp); sn += xhalf(sn);
  const float rp = __builtin_amdgcn_rcpf(sp), rn = __builtin_amdgcn_rcpf(sn);
#pragma unroll
  for (int s = 0; s < 2; ++s) {
    u32x4 a, b;
#pragma unroll
    for (int i = 0; i < 4; ++i) {
      a[i] = pk_bf16(ep[8 * s + 2 * i] * rp, ep[8 * s + 2 * i + 1] * rp);
      b[i] = pk_bf16(en[8 * s + 2 * i] * rn, en[8 * s + 2 * i + 1] * rn);
    }
    pos[s] = __builtin_bit_cast(bf16x8, a); neg[s] = __builtin_bit_cast(bf16x8, b);
  }
}

constexpr int G1_STAGE = 40960;
constexpr int L_WPHI = 122880;
constexpr int L_A1 = 126976;
constexpr int CV_WAVE = 20480;
constexpr int L_E1 = 81920;
constexpr int L_OUT = 131072;
constexpr int L_A3 = 147456;
DI int win_addr(int m0) {
  return ((m0 >> 3) << 6) + ((((m0 >> 1) & 3) ^ ((m0 >> 4) & 3)) << 4) + ((m0 & 1) << 3);
}
#define HBAR() do { __builtin_amdgcn_s_barrier(); asm volatile("" ::: "memory"); SCHED0(); } while (0)

DI void role_h(int b, int wave, char* sm, const u16* __restrict__ q, const u16* __restrict__ k, const u16* __restrict__ v,
               const u16* __restrict__ shk, const u16* __restrict__ hk, const u16* __restrict__ Dv, const u16* __restrict__ x,
               const u16* __restrict__ W, const u16* __restrict__ wo, char* ws, u16* __restrict__ out) {
  const int lane = threadIdx.x & 63;
  const int xcd = b & 7, a = xcd * 32 + (b >> 3), grp = a >> 4, idx = a & 15;
  const rsrc_t rw = mk_rsrc(ws, WS_USED);
  int hb_ = 0; (void)hb_;
  ST(1, 0);
  const unsigned epoch = __hip_atomic_load((GAS const unsigned*)(ws + FL_EPOCH), __ATOMIC_RELAXED, __HIP_MEMORY_SCOPE_AGENT);
  const int cgp = lane & 7, rg = lane >> 3;
  const int ch0 = grp * 64 + cgp * 8, r0 = idx * 128 + wave * 32 + rg * 4;
  const int ch = grp * 64 + idx * 4 + wave;
  u32x4 hv[4]; u32x2 hp[4];
  char* tl_ = sm + L_A1 + wave * 4096;
  const rsrc_t rt = (wave & 1) ? mk_rsrc(W, 1024 * 3072 * 2) : mk_rsrc(x, 2048 * 1024 * 2);
  unsigned tvo;
  {
    const int bi = b >> 3, l48 = lane < 48 ? lane : 0;
    tvo = (wave & 1) ? ((bi * 4 + l48 / 12) * 3072 + ((l48 % 12) >> 2) * 1024 + (l48 & 3) * 64 + (xcd >> 1) * 256) * 2
                     : (((xcd & 1) * 1024 + bi * 32 + (lane >> 1)) * 1024 + (lane & 1) * 64) * 2;
  }
  const unsigned tstep = (wave & 1) ? 128 * 3072 * 2 : 256;
#define TD 4
  auto touch = [&](int t) {
    if (!(t & 1) && t + TD < 16 && ((t >> 1) & 1) == (wave >> 1))
      __builtin_amdgcn_raw_ptr_buffer_load_lds(rt, (LAS void*)(sm + L_A3 + wave * 256), 4, tvo, ((t + TD) >> 1) * tstep, 0, 0);
  };
  if (TD >= 4) touch(-2);
  SCHED0();
  HBAR();
  u32x4 kk[7], vv[4], sk[4];
#pragma unroll
  for (int i = 0; i < 7; ++i) {
    const int r = r0 - 3 + i;
    kk[i] = *(const u32x4*)(k + (size_t)(r < 0 ? 0 : r) * 1024 + ch0);
  }
#pragma unroll
  for (int i = 0; i < 4; ++i) vv[i] = *(const u32x4*)(v + (size_t)(r0 + i) * 1024 + ch0);
#pragma unroll
  for (int i = 0; i < 4; ++i) sk[i] = *(const u32x4*)(shk + ch0 * 4 + i * 8);
  touch(0); HBAR(); HBAR(); touch(2); HBAR(); HBAR();
  {
    const u16* hrow = hk + (size_t)ch * 2048;
#pragma unroll
    for (int bb = 0; bb < 4; ++bb) {
      const int e0 = bb * 512 + lane * 8;
      hv[bb] = *(const u32x4*)(hrow + e0);
      hp[bb] = *(const u32x2*)(hrow + (e0 > 0 ? e0 - 4 : 0));
    }
  }
  const unsigned dvraw = Dv[ch];
  touch(4); HBAR(); HBAR();
#pragma unroll
  for (int i = 0; i < 3; ++i) if (r0 - 3 + i < 0) kk[i] = u32x4{0u, 0u, 0u, 0u};
#pragma unroll
  for (int e2 = 0; e2 < 4; ++e2) {
    f32x2 w[4];
    w[0] = f32x2{bf_lo(sk[e2][0]), bf_lo(sk[e2][2])}; w[1] = f32x2{bf_hi(sk[e2][0]), bf_hi(sk[e2][2])};
    w[2] = f32x2{bf_lo(sk[e2][1]), bf_lo(sk[e2][3])}; w[3] = f32x2{bf_hi(sk[e2][1]), bf_hi(sk[e2][3])};
    f32x2 kf[7], o[4];
#pragma unroll
    for (int i = 0; i < 7; ++i) kf[i] = f32x2{bf_lo(kk[i][e2]), bf_hi(kk[i][e2])};
#pragma unroll
    for (int i = 0; i < 4; ++i) {
      f32x2 acc = kf[i] * w[3];
      acc = __builtin_elementwise_fma(kf[i + 1], w[2], acc);
      acc = __builtin_elementwise_fma(kf[i + 2], w[1], acc);
      acc = __builtin_elementwise_fma(kf[i + 3], w[0], acc);
      const unsigned ks = pk_bf16(acc[0], acc[1]);
      o[i] = f32x2{bf_lo(ks), bf_hi(ks)} * f32x2{bf_lo(vv[i][e2]), bf_hi(vv[i][e2])};
    }
    u32x2 wa = {pk_bf16(o[0][0], o[1][0]), pk_bf16(o[2][0], o[3][0])}, wb = {pk_bf16(o[0][1], o[1][1]), pk_bf16(o[2][1], o[3][1])};
    *(u32x2*)(tl_ + (cgp * 8 + 2 * e2) * 64 + ((rg ^ (cgp & 6)) << 3)) = wa;
    *(u32x2*)(tl_ + (cgp * 8 + 2 * e2 + 1) * 64 + ((rg ^ (cgp & 6)) << 3)) = wb;
    if (e2 & 1) { touch(6 + (e2 >> 1)); HBAR(); }
  }
#pragma unroll
  for (int it = 0; it < 4; ++it) {
    const int c = it * 16 + (lane >> 2), ck = lane & 3;
    const u32x4 d = *(const u32x4*)(tl_ + c * 64 + ((ck ^ ((c >> 4) & 3)) << 4));
    st16_sc1(d, rw, (grp * 64 + c) * 4096 + (idx * 128 + wave * 32 + ck * 8) * 2, WS_XT);
  }
  ST(1, 1);
  touch(8); HBAR(); HBAR();
  const u64 fv = flag_value(__builtin_amdgcn_readfirstlane(epoch));
  publish(ws, FL_A1 + (grp * 64 + idx * 4 + wave) * 8, fv, lane);
  ST(1, 2);
  touch(10); HBAR(); HBAR(); HBAR(); HBAR();
  poll(ws, FL_A1 + grp * 512, 64, fv, lane);
  u32x4 xv[4];
#pragma unroll
  for (int bb = 0; bb < 4; ++bb) xv[bb] = ld16_sc1(rw, ch * 4096 + bb * 1024 + lane * 16, WS_XT);
  ST(1, 3);
  HBAR(); HBAR();
  ST(1, 4);
  char* wl = sm + wave * CV_WAVE;
  char* xl = wl + 16384;
  const float dcoef = bf_lo(dvraw);
  if (lane == 0) { hp[0][0] = 0u; hp[0][1] = 0u; }
#pragma unroll
  for (int bb = 0; bb < 4; ++bb) {
    *(u32x4*)(xl + (lane & 1) * 2048 + (bb * 32 + (lane >> 1)) * 16) = xv[bb];
    const unsigned dm2 = hp[bb][0], dm1 = hp[bb][1], d0 = hv[bb][0], d1 = hv[bb][1], d2 = hv[bb][2], d3 = hv[bb][3];
    unsigned R[10];
    R[0] = __builtin_amdgcn_perm(dm2, dm1, 0x07060100u);
    R[1] = __builtin_amdgcn_alignbit(dm1, dm1, 16);
    R[2] = __builtin_amdgcn_perm(dm1, d0, 0x07060100u);
    R[3] = __builtin_amdgcn_alignbit(d0, d0, 16);
    R[4] = __builtin_amdgcn_perm(d0, d1, 0x07060100u);
    R[5] = __builtin_amdgcn_alignbit(d1, d1, 16);
    R[6] = __builtin_amdgcn_perm(d1, d2, 0x07060100u);
    R[7] = __builtin_amdgcn_alignbit(d2, d2, 16);
    R[8] = __builtin_amdgcn_perm(d2, d3, 0x07060100u);
    R[9] = __builtin_amdgcn_alignbit(d3, d3, 16);
    const int i = bb * 64 + lane, sw = (i >> 1) & 3;
#pragma unroll
    for (int r = 0; r < 4; ++r) {
      u32x4 w = {R[2 * r + 2], R[2 * r], R[2 * r + 3], R[2 * r + 1]};
      *(u32x4*)(wl + i * 64 + ((r ^ sw) << 4)) = w;
    }
  }
  ST(1, 5);
  const int ca = lane & 15, g = lane >> 4;
  const int off = (g & 1) * 16 + (g >> 1) * 8;
  const int cb = 2 * ca + 1 - (off >> 3);
  const int xb0 = 16384 + (cb & 1) * 2048 + ((cb >> 1) << 4);
  f32x4 acc[8];
#pragma unroll
  for (int c = 0; c < 8; ++c) acc[c] = f32x4{0.f, 0.f, 0.f, 0.f};
  bf16x8 A[2][8], B[2][8];
  const bf16x8 zero8 = {0, 0, 0, 0, 0, 0, 0, 0};
  const int wbase = wave * CV_WAVE;
  auto ld = [&](int set, int t0, auto FIRST) {
    const int m0 = 32 * t0 + ca + off - 8;
    const char* pb = wl + xb0 - t0 * 32;
    if constexpr (FIRST.value) {
      const s16x4 z4 = {0, 0, 0, 0};
      s16x4 a0 = *(const s16x4*)(wl + win_addr(m0 < 0 ? 0 : m0)), a1 = *(const s16x4*)(wl + win_addr(m0 - 4 < 0 ? 0 : m0 - 4));
      A[set][0] = cat8(m0 < 0 ? z4 : a0, m0 - 4 < 0 ? z4 : a1);
    }
    const int pa0 = wbase + win_addr(m0 + 256) - 2048, pa1 = wbase + win_addr(m0 + 252) - 2048;
#pragma unroll
    for (int i = 0; i < 8; ++i) {
      B[set][7 - i] = *(const bf16x8*)(pb + (7 - i) * 256);
      if (!(FIRST.value && i == 0)) {
        int q0 = pa0 + i * 2048, q1 = pa1 + i * 2048;
        asm volatile("" : "+v"(q0), "+v"(q1));
        A[set][i] = cat8(*(const s16x4*)(sm + q0), *(const s16x4*)(sm + q1));
      }
    }
  };
  auto mm = [&](int set, int t0) {
    const bf16x8 b0 = (cb - 4 * t0 < 0) ? zero8 : B[set][0];
#pragma unroll
    for (int t1 = 0; t1 < 8; ++t1)
#pragma unroll
      for (int i = 7 - t1; i >= 0; --i) acc[t1 + i] = mfma16(A[set][t1], i == 0 ? b0 : B[set][i], acc[t1 + i]);
  };
  ld(0, 0, T_{});
  SCHED0();
#define CV_SCHED() do { _Pragma("unroll") for (int z = 0; z < 24; ++z) { SGB(0x8, 1); SGB(0x100, 1); } SGB(0x8, 12); SCHED0(); \
    asm volatile("" : "+v"(acc[0]), "+v"(acc[1]), "+v"(acc[2]), "+v"(acc[3]), "+v"(acc[4]), "+v"(acc[5]), "+v"(acc[6]), "+v"(acc[7])); } while (0)
#pragma unroll 1
  for (int t0 = 0; t0 < 6; t0 += 2) {
    ld(1, t0 + 1, F_{}); mm(0, t0); CV_SCHED();
    ld(0, t0 + 2, F_{}); mm(1, t0 + 1); CV_SCHED();
  }
  ld(1, 7, F_{}); mm(0, 6); CV_SCHED();
  mm(1, 7);
  ST(1, 6);
  u32x4 qq[4];
#pragma unroll
  for (int i = 0; i < 4; ++i) qq[i] = *(const u32x4*)(q + (size_t)(r0 + i) * 1024 + ch0);
  char* yl = sm + L_A3 + wave * 4096;
  {
    const int xe = 16384 + (g >> 1) * 2048 + (ca << 4) + (g & 1) * 8;
#pragma unroll
    for (int c = 0; c < 8; ++c) {
      const u32x2 kv = *(const u32x2*)(wl + xe + c * 256);
      const unsigned y01 = pk_bf16(acc[c][0], acc[c][1]), y23 = pk_bf16(acc[c][2], acc[c][3]);
      const unsigned t01 = pk_bf16(bf_lo(kv[0]) * dcoef, bf_hi(kv[0]) * dcoef), t23 = pk_bf16(bf_lo(kv[1]) * dcoef, bf_hi(kv[1]) * dcoef);
      u32x2 o = {pk_bf16(bf_lo(y01) + bf_lo(t01), bf_hi(y01) + bf_hi(t01)), pk_bf16(bf_lo(y23) + bf_lo(t23), bf_hi(y23) + bf_hi(t23))};
      *(u32x2*)(yl + c * 512 + ca * 32 + g * 8) = o;
    }
#pragma unroll
    for (int it = 0; it < 4; ++it) st16_sc1(*(const u32x4*)(yl + it * 1024 + lane * 16), rw, ch * 4096 + it * 1024 + lane * 16, WS_YT);
  }
  WAIT_L0();
  {
    const rsrc_t ro = mk_rsrc(wo, 1024 * 1024 * 2);
    const int j = (xcd >> 1) * 4 + (b >> 6);
    const unsigned wosrc = ((wave * 160 + ((lane >> 2) & 7)) * 1024 + j * 64 + (lane >> 5) * 32 + (lane & 3) * 8) * 2;
#pragma unroll
    for (int p = 0; p < 20; ++p) blds16(ro, wosrc, p * 16384, wl + p * 1024);
  }
  publish<20>(ws, FL_Y + (grp * 64 + idx * 4 + wave) * 8, fv, lane);
  ST(1, 7);
  WAIT_V(0);
  HBAR();
  poll(ws, FL_Y + grp * 512, 64, fv, lane);
  ST(1, 8);
  {
    u32x4 yy[4];
#pragma unroll
    for (int it = 0; it < 4; ++it) {
      const int c = it * 16 + (lane >> 2), ck = lane & 3;
      yy[it] = ld16_sc1(rw, (grp * 64 + c) * 4096 + (idx * 128 + wave * 32 + ck * 8) * 2, WS_YT);
    }
#pragma unroll
    for (int it = 0; it < 4; ++it) {
      const int c = it * 16 + (lane >> 2), ck = lane & 3;
      *(u32x4*)(yl + c * 64 + ((ck ^ ((c >> 4) & 3)) << 4)) = yy[it];
    }
    u32x2 y[8];
#pragma unroll
    for (int e = 0; e < 8; ++e) y[e] = *(const u32x2*)(yl + (cgp * 8 + e) * 64 + ((rg ^ (cgp & 6)) << 3));
#pragma unroll
    for (int i = 0; i < 4; ++i) {
      u32x4 o;
#pragma unroll
      for (int e2 = 0; e2 < 4; ++e2) {
        const unsigned ya = y[2 * e2][i >> 1], yb = y[2 * e2 + 1][i >> 1];
        const float fa = (i & 1) ? bf_hi(ya) : bf_lo(ya), fb = (i & 1) ? bf_hi(yb) : bf_lo(yb);
        o[e2] = pk_bf16(bf_lo(qq[i][e2]) * fa, bf_hi(qq[i][e2]) * fb);
      }
      *(u32x4*)(out + (size_t)(r0 + i) * 2048 + ch0) = o;
    }
  }
  ST(1, 9);
}

DI void role_g(int bid, int wave, char* sm, const u16* __restrict__ x, const u16* __restrict__ W, const u16* __restrict__ wphi,
               const u16* __restrict__ wo, char* ws, u16* __restrict__ out) {
  int lane = threadIdx.x & 63;
  const int xcd = bid & 7, idx = bid >> 3;
  const int rb = (xcd & 1) * 8 + (idx & 7), j = (xcd >> 1) * 4 + (idx >> 3);
  const int wm = wave & 1, wn = wave >> 1;
  int h = lane >> 5, l31 = lane & 31;
  int blk = (lane >> 4) & 1, q4 = (lane & 15) >> 2, p4 = lane & 3;
  int troff = q4 * 64 + (2 * blk + (p4 >> 1)) * 16 + (p4 & 1) * 8;
  const int head = 2 * j + wn;
#define RELANE() do { asm volatile("" : "+v"(lane)); h = lane >> 5; l31 = lane & 31; blk = (lane >> 4) & 1; q4 = (lane & 15) >> 2; p4 = lane & 3; \
    troff = q4 * 64 + (2 * blk + (p4 >> 1)) * 16 + (p4 & 1) * 8; SCHED0(); } while (0)
  const rsrc_t rx = mk_rsrc(x, 2048 * 1024 * 2), rW = mk_rsrc(W, 1024 * 3072 * 2), rp = mk_rsrc(wphi, 65536), rw = mk_rsrc(ws, WS_USED);
  ST(0, 0);
  f32x16 zero;
#pragma unroll
  for (int i = 0; i < 16; ++i) zero[i] = 0.f;
  volatile LAS unsigned* hs = (volatile LAS unsigned*)(sm + L_A1 + 16384);
  if (wm == 0 && lane == 0) hs[wn] = 0u;
  f32x16 acc[2][3];
  {
    unsigned xs[4], wsrc[6];
#pragma unroll
    for (int i = 0; i < 4; ++i) {
      const int p = wave * 4 + i, row = p * 8 + (lane >> 3), c = (lane & 7) ^ ((row >> 1) & 7);
      xs[i] = ((rb * 128 + row) * 1024 + c * 8) * 2;
    }
#pragma unroll
    for (int i = 0; i < 6; ++i) {
      const int p = wave * 6 + i, kg = p / 3, seg = p % 3;
      wsrc[i] = ((kg * 8 + ((lane >> 2) & 7)) * 3072 + seg * 1024 + j * 64 + (lane >> 5) * 32 + (lane & 3) * 8) * 2;
    }
    auto piece = [&](int t, int buf, int i) {
      char* bb = sm + buf * G1_STAGE;
      if (i < 4) blds16(rx, xs[i], t * 128, bb + (wave * 4 + i) * 1024);
      else blds16(rW, wsrc[i - 4], t * (64 * 3072 * 2), bb + 16384 + (wave * 6 + i - 4) * 1024);
    };
#pragma unroll
    for (int i = 0; i < 10; ++i) piece(0, 0, i);
    blds16(rp, (j * 2048 + wave * 512 + lane * 8) * 2, 0, sm + L_WPHI + wave * 1024);
#pragma unroll
    for (int i = 0; i < 10; ++i) piece(1, 1, i);
#pragma unroll
    for (int i = 0; i < 4; ++i) piece(2, 2, i);
    int xoff[4];
    {
      const int row = wm * 64 + l31, sw = (row >> 1) & 7;
#pragma unroll
      for (int s = 0; s < 4; ++s) xoff[s] = row * 128 + (((2 * s + h) ^ sw) << 4);
    }
    const int woff = 16384 + h * 3072 + wn * 512 + troff;
#pragma unroll
    for (int a2 = 0; a2 < 2; ++a2)
#pragma unroll
      for (int b2 = 0; b2 < 3; ++b2) acc[a2][b2] = zero;
    bf16x8 xf[2][2], wf[2][3];
    auto rd = [&](int set, const char* bb, int s) {
      xf[set][0] = *(const bf16x8*)(bb + xoff[s]);
#pragma unroll
      for (int gq = 0; gq < 3; ++gq) wf[set][gq] = cat8(tr_read(bb + woff + s * 6144 + gq * 1024), tr_read(bb + woff + s * 6144 + gq * 1024 + 256));
      xf[set][1] = *(const bf16x8*)(bb + xoff[s] + 4096);
    };
    auto mm = [&](int set) {
#pragma unroll
      for (int ts = 0; ts < 2; ++ts) {
        acc[ts][0] = mfma32(wf[set][0], xf[set][ts], acc[ts][0]);
        acc[ts][1] = mfma32(wf[set][1], xf[set][ts], acc[ts][1]);
        acc[ts][2] = mfma32(xf[set][ts], wf[set][2], acc[ts][2]);
      }
    };
    auto pin = [&]() { asm volatile("" : "+v"(acc[0][0]), "+v"(acc[0][1]), "+v"(acc[0][2]), "+v"(acc[1][0]), "+v"(acc[1][1]), "+v"(acc[1][2])); };
#define G1_SCHED(NV) do { SGB(0x100, 3); SGB(0x8, 1); SGB(0x100, 2); SGB(0x8, 1); SGB(0x100, 3); SGB(0x8, 1); \
    if ((NV) > 0) SGB(0x10, 1); SGB(0x8, 1); if ((NV) > 1) SGB(0x10, 1); SGB(0x8, 1); if ((NV) > 2) SGB(0x10, 1); SGB(0x8, 1); if ((NV) > 3) SGB(0x10, 1); SCHED0(); } while (0)
    ST(0, 1);
    WAIT_V(15); BAR();
    ST(0, 2);
    rd(0, sm, 0);
    SCHED0();
    int cur = 0, nxt = 1, nx2 = 2;
    auto step = [&](int t, auto BC, auto AA, auto WN, auto RN) {
      const char* bb = sm + cur * G1_STAGE;
      rd(1, bb, 1);
      if constexpr (BC.value) { piece(t + 2, nx2, 4); piece(t + 2, nx2, 5); piece(t + 2, nx2, 6); }
      mm(0); G1_SCHED(BC.value ? 3 : 0); pin();
      rd(0, bb, 2);
      if constexpr (BC.value) { piece(t + 2, nx2, 7); piece(t + 2, nx2, 8); piece(t + 2, nx2, 9); }
      mm(1); G1_SCHED(BC.value ? 3 : 0); pin();
      rd(1, bb, 3); mm(0); G1_SCHED(0); pin();
      WAIT_V(WN.value);
      BAR();
      SCHED0();
      if constexpr (RN.value) rd(0, sm + nxt * G1_STAGE, 0);
      if constexpr (AA.value) { piece(t + 3, cur, 0); piece(t + 3, cur, 1); piece(t + 3, cur, 2); piece(t + 3, cur, 3); }
      mm(1); G1_SCHED(AA.value ? 4 : 0); pin();
      const int o = cur; cur = nxt; nxt = nx2; nx2 = o;
    };
#pragma unroll 1
    for (int t = 0; t < 13; ++t) step(t, T_{}, T_{}, IC<10>{}, T_{});
    step(13, T_{}, F_{}, IC<10>{}, T_{});
    step(14, F_{}, F_{}, IC<0>{}, T_{});
    step(15, F_{}, F_{}, IC<0>{}, F_{});
  }
  ST(0, 3);
  RELANE();
  const unsigned epoch = __hip_atomic_load((GAS const unsigned*)(ws + FL_EPOCH), __ATOMIC_RELAXED, __HIP_MEMORY_SCOPE_AGENT);
  bf16x8 wp[2];
  {
    const char* wb = sm + L_WPHI + wn * 2048 + h * 256 + troff;
#pragma unroll
    for (int s = 0; s < 2; ++s) wp[s] = cat8(tr_read(wb + s * 1024), tr_read(wb + s * 1024 + 512));
  }
  const rsrc_t ro = mk_rsrc(wo, 1024 * 1024 * 2);
  u64 fv;
  {
    char* img = sm + L_E1 + wave * 8192;
#pragma unroll
    for (int ts = 0; ts < 2; ++ts) {
      f32x16 pk = mfma32(wp[0], pack8(acc[ts][1], 0), zero);
      pk = mfma32(wp[1], pack8(acc[ts][1], 1), pk);
      bf16x8 pos[2], neg[2];
      dual_softmax(pk, pos, neg);
      const int row = ts * 32 + l31, sw = (row >> 1) & 7;
#pragma unroll
      for (int gq = 0; gq < 4; ++gq) {
        const u32x4 pp = __builtin_bit_cast(u32x4, pos[gq >> 1]), nn = __builtin_bit_cast(u32x4, neg[gq >> 1]);
        u32x2 aa = {pp[(gq & 1) * 2], pp[(gq & 1) * 2 + 1]}, bb = {nn[(gq & 1) * 2], nn[(gq & 1) * 2 + 1]};
        const int o = row * 64 + (((2 * gq + h) ^ sw) << 3);
        *(u32x2*)(img + o) = aa;
        *(u32x2*)(img + 4096 + o) = bb;
      }
    }
    f32x16 kvacc[2], zacc[2];
    kvacc[0] = zero; kvacc[1] = zero; zacc[0] = zero; zacc[1] = zero;
    bf16x8 ones;
#pragma unroll
    for (int i = 0; i < 8; ++i) ones[i] = (short)0x3f80;
#pragma unroll
    for (int ts = 0; ts < 2; ++ts)
#pragma unroll
      for (int s = 0; s < 2; ++s) {
        const bf16x8 vb = pack8(acc[ts][2], s);
        const int r0 = ts * 32 + 16 * s + 4 * h + q4, r1 = r0 + 8;
        const int o0 = r0 * 64 + (((4 * blk + p4) ^ ((r0 >> 1) & 7)) << 3), o1 = r1 * 64 + (((4 * blk + p4) ^ ((r1 >> 1) & 7)) << 3);
#pragma unroll
        for (int tl = 0; tl < 2; ++tl) {
          const bf16x8 pa = cat8(tr_read(img + tl * 4096 + o0), tr_read(img + tl * 4096 + o1));
          kvacc[tl] = mfma32(pa, vb, kvacc[tl]);
          zacc[tl] = mfma32(pa, ones, zacc[tl]);
        }
      }
    fv = flag_value(__builtin_amdgcn_readfirstlane(epoch));
    asm volatile("" ::"s"(fv));
    char* red = sm + L_E1 + (wn * 2 + 1) * 8192;
    char* zred = sm + L_E1 + 32768 + wn * 256;
    if (wm == 1) {
#pragma unroll
      for (int tl = 0; tl < 2; ++tl)
#pragma unroll
        for (int qd = 0; qd < 4; ++qd) {
          f32x4 t = {kvacc[tl][4 * qd], kvacc[tl][4 * qd + 1], kvacc[tl][4 * qd + 2], kvacc[tl][4 * qd + 3]};
          *(f32x4*)(red + (tl * 4 + qd) * 1024 + lane * 16) = t;
        }
      if (l31 == 0) {
#pragma unroll
        for (int tl = 0; tl < 2; ++tl)
#pragma unroll
          for (int qd = 0; qd < 4; ++qd) {
            f32x4 z = {zacc[tl][4 * qd], zacc[tl][4 * qd + 1], zacc[tl][4 * qd + 2], zacc[tl][4 * qd + 3]};
            *(f32x4*)(zred + ((tl * 2 + h) * 4 + qd) * 16) = z;
          }
      }
      WAIT_L0();
      if (lane == 0) hs[wn] = 1u;
    } else {
#pragma unroll 1
      for (unsigned spins = 0; spins < (1u << 24) && hs[wn] == 0u; ++spins) __builtin_amdgcn_s_sleep(1);
      asm volatile("" ::: "memory");
#pragma unroll
      for (int tl = 0; tl < 2; ++tl)
#pragma unroll
        for (int qd = 0; qd < 4; ++qd) {
          f32x4 t = *(const f32x4*)(red + (tl * 4 + qd) * 1024 + lane * 16);
#pragma unroll
          for (int i = 0; i < 4; ++i) t[i] += kvacc[tl][4 * qd + i];
          st16_sc1(__builtin_bit_cast(u32x4, t), rw, ((rb * 32 + head) * 8 + tl * 4 + qd) * 1024 + lane * 16, WS_KVP);
        }
      if (l31 == 0) {
#pragma unroll
        for (int tl = 0; tl < 2; ++tl)
#pragma unroll
          for (int qd = 0; qd < 4; ++qd) {
            f32x4 z = *(const f32x4*)(zred + ((tl * 2 + h) * 4 + qd) * 16);
#pragma unroll
            for (int i = 0; i < 4; ++i) z[i] += zacc[tl][4 * qd + i];
            st16_sc1(__builtin_bit_cast(u32x4, z), rw, (rb * 32 + head) * 256 + ((tl * 2 + h) * 4 + qd) * 16, WS_ZP);
          }
      }
    }
  }
  ST(0, 4);
  bf16x8 ph[2][4];
  {
    f32x16 pq = mfma32(wp[0], pack8(acc[0][0], 0), zero);
    pq = mfma32(wp[1], pack8(acc[0][0], 1), pq);
    dual_softmax(pq, ph[0], ph[0] + 2);
  }
  SCHED0();
  if (wm == 0) publish(ws, FL_P + (head * 16 + rb) * 8, fv, lane);
  {
    f32x16 pq = mfma32(wp[0], pack8(acc[1][0], 0), zero);
    pq = mfma32(wp[1], pack8(acc[1][0], 1), pq);
    dual_softmax(pq, ph[1], ph[1] + 2);
  }
  ST(0, 5);
  RELANE();
  if (wave == 1) {
    const int rhead = 2 * j + (rb >> 3), row = rb & 7;
    poll(ws, FL_P + rhead * 128, 16, fv, lane);
    ST(0, 6);
    u32x4 e[16];
#pragma unroll
    for (int r = 0; r < 16; ++r) e[r] = ld16_sc1(rw, (rhead * 8 + row) * 1024 + lane * 16, WS_KVP + r * 262144);
    float ze[16];
    if (row == 0) {
#pragma unroll
      for (int r = 0; r < 16; ++r) ze[r] = __uint_as_float(__builtin_amdgcn_raw_buffer_load_b32(rw, rhead * 256 + lane * 4, WS_ZP + r * 8192, 16));
    }
    f32x4 s = {0.f, 0.f, 0.f, 0.f};
#pragma unroll
    for (int r = 0; r < 16; ++r) s += __builtin_bit_cast(f32x4, e[r]);
    u32x2 o = {pk_bf16(s[0], s[1]), pk_bf16(s[2], s[3])};
    __builtin_amdgcn_raw_buffer_store_b64(o, rw, (rhead * 4 + (row >> 1)) * 1024 + lane * 16 + (row & 1) * 8, WS_KVF, 16);
    if (row == 0) {
      float zs = 0.f;
#pragma unroll
      for (int r = 0; r < 16; ++r) zs += ze[r];
      const float zn = __shfl_down(zs, 1);
      if ((lane & 1) == 0) __builtin_amdgcn_raw_buffer_store_b32(pk_bf16(zs, zn), rw, rhead * 128 + lane * 2, WS_ZF, 16);
    }
    publish(ws, FL_R + (j * 16 + rb) * 8, fv, lane);
    ST(0, 7);
  }
  {
    poll(ws, FL_R + (j * 16 + wn * 8) * 8, 8, fv, lane);
    ST(0, 8);
    bf16x8 kvf[4], zf[4];
#pragma unroll
    for (int i = 0; i < 4; ++i) {
      kvf[i] = __builtin_bit_cast(bf16x8, ld16_sc1(rw, (head * 4 + i) * 1024 + lane * 16, WS_KVF));
      zf[i] = __builtin_bit_cast(bf16x8, ld16_sc1(rw, head * 128 + ((i >> 1) * 2 + h) * 32 + (i & 1) * 16, WS_ZF));
    }
#pragma unroll
    for (int ts = 0; ts < 2; ++ts) {
      f32x16 num = zero, den = zero;
#pragma unroll
      for (int i = 0; i < 4; ++i) { num = mfma32(kvf[i], ph[ts][i], num); den = mfma32(zf[i], ph[ts][i], den); }
      const float dn = rbf(rbf(den[0]) + 1e-6f);
      const float rdn = 1.0f / dn;
      f32x16 o;
#pragma unroll
      for (int i = 0; i < 8; ++i) {
        const unsigned u = pk_bf16(num[2 * i], num[2 * i + 1]);
        o[2 * i] = bf_lo(u) * rdn; o[2 * i + 1] = bf_hi(u) * rdn;
      }
      const unsigned dst = (((rb * 4 + wm * 2 + ts) * 32 + head) * 2) * 1024 + lane * 16;
      st16_sc1(__builtin_bit_cast(u32x4, pack8(o, 0)), rw, dst, WS_OF);
      st16_sc1(__builtin_bit_cast(u32x4, pack8(o, 1)), rw, dst + 1024, WS_OF);
    }
    poll(ws, FL_R + (j * 16 + (wn ^ 1) * 8) * 8, 8, fv, lane);
    publish(ws, FL_O + (rb * 64 + wm * 32 + j * 2 + wn) * 8, fv, lane);
    {
      const unsigned wosrc = (((80 + wave * 12) * 8 + ((lane >> 2) & 7)) * 1024 + j * 64 + (lane >> 5) * 32 + (lane & 3) * 8) * 2;
#pragma unroll
      for (int p = 0; p < 12; ++p) blds16(ro, wosrc, p * 16384, sm + (80 + wave * 12 + p) * 1024);
    }
  }
  ST(0, 9);
  {
    poll(ws, FL_O + (rb * 64 + (wave >> 1) * 32) * 8, 32, fv, lane);
    ST(0, 10);
    const unsigned osrc = (rb * 4 + wave) * 65536 + lane * 16;
    bf16x8 of[32];
#pragma unroll
    for (int i = 0; i < 32; ++i) of[i] = __builtin_bit_cast(bf16x8, ld16_sc1(rw, osrc + i * 1024, WS_OF));
    f32x16 oa[2];
    oa[0] = zero; oa[1] = zero;
    bf16x8 wf[2][2];
    const char* wb = sm + h * 256 + troff;
    auto rd = [&](int set, const char* bb) {
      wf[set][0] = cat8(tr_read(bb), tr_read(bb + 1024));
      wf[set][1] = cat8(tr_read(bb + 512), tr_read(bb + 1536));
    };
    BAR();
    ST(0, 11);
    rd(0, wb);
    SCHED0();
    auto chunk = [&](int c, auto RELOAD, auto LAST) {
      const char* bb = wb + c * 65536;
#pragma unroll
      for (int i = 0; i < 32; ++i) {
        if (!(i == 31 && LAST.value)) rd((i + 1) & 1, bb + (i + 1) * 2048);
        oa[0] = mfma32(wf[i & 1][0], of[i], oa[0]);
        oa[1] = mfma32(wf[i & 1][1], of[i], oa[1]);
        if constexpr (RELOAD.value) of[i] = __builtin_bit_cast(bf16x8, ld16_sc1(rw, osrc + i * 1024, WS_OF + 32768));
        SGB(0x100, 2); SGB(0x8, 1); SGB(0x100, 2); SGB(0x8, 1); SGB(0x10, 1); SCHED0();
        asm volatile("" : "+v"(oa[0]), "+v"(oa[1]));
      }
    };
    chunk(0, T_{}, F_{});
    chunk(1, F_{}, T_{});
    ST(0, 12);
    RELANE();
    char* ot = sm + L_OUT + wave * 4096;
#pragma unroll
    for (int gq = 0; gq < 2; ++gq)
#pragma unroll
      for (int qd = 0; qd < 4; ++qd) {
        u32x2 w = {pk_bf16(oa[gq][4 * qd], oa[gq][4 * qd + 1]), pk_bf16(oa[gq][4 * qd + 2], oa[gq][4 * qd + 3])};
        *(u32x2*)(ot + l31 * 128 + (((4 * gq + qd) ^ (l31 & 7)) << 4) + h * 8) = w;
      }
#pragma unroll
    for (int it = 0; it < 4; ++it) {
      const int row = it * 8 + (lane >> 3), c = lane & 7;
      const u32x4 d = *(const u32x4*)(ot + row * 128 + ((c ^ (row & 7)) << 4));
      *(u32x4*)(out + (size_t)(rb * 128 + wave * 32 + row) * 2048 + 1024 + j * 64 + c * 8) = d;
    }
  }
  if (bid == 0 && threadIdx.x == 0) __hip_atomic_store((GAS unsigned*)(ws + FL_EPOCH), epoch + 1u, __ATOMIC_RELAXED, __HIP_MEMORY_SCOPE_AGENT);
  ST(0, 13);
}

__global__ __launch_bounds__(512, 2) void fused_kernel(const u16* q, const u16* k, const u16* v, const u16* shk, const u16* hk, const u16* Dv,
                                                        const u16* x, const u16* W, const u16* wphi, const u16* wo, char* ws, u16* out) {
  extern __shared__ __attribute__((aligned(16))) char sm[];
  const int wave = __builtin_amdgcn_readfirstlane(threadIdx.x >> 6);
  if (wave < 4) role_g(blockIdx.x, wave, sm, x, W, wphi, wo, ws, out);
  else role_h(blockIdx.x, wave - 4, sm, q, k, v, shk, hk, Dv, x, W, wo, ws, out);
}

extern "C" void kernel_launch(void* const* d_in, const int* in_sizes, int n_in, void* d_out, int out_size, void* d_ws, size_t ws_size,
                              hipStream_t stream) {
  (void)hipFuncSetAttribute((const void*)fused_kernel, hipFuncAttributeMaxDynamicSharedMemorySize, LDS_BYTES);
  fused_kernel<<<256, 512, LDS_BYTES, stream>>>((const u16*)d_in[0], (const u16*)d_in[1], (const u16*)d_in[2], (const u16*)d_in[3],
                                                (const u16*)d_in[4], (const u16*)d_in[5], (const u16*)d_in[6], (const u16*)d_in[7],
                                                (const u16*)d_in[8], (const u16*)d_in[9], (char*)d_ws, (u16*)d_out);
}
```

```cpp
#define STAMP
#define MEAS
#define W0ONLY 0
#define KA 0
#define IA 0
#define AMIN 1
#define KB 0
#define IB 0
#define BMAX 0
#include <hip/hip_runtime.h>
#include <stdint.h>
#include <type_traits>

typedef short bf16x8 __attribute__((ext_vector_type(8)));
typedef short s16x4 __attribute__((ext_vector_type(4)));
typedef float f32x4 __attribute__((ext_vector_type(4)));
typedef float f32x2 __attribute__((ext_vector_type(2)));
typedef float f32x16 __attribute__((ext_vector_type(16)));
typedef unsigned u32x4 __attribute__((ext_vector_type(4)));
typedef unsigned u32x2 __attribute__((ext_vector_type(2)));
typedef __bf16 bfx2 __attribute__((ext_vector_type(2)));
typedef unsigned short u16;
typedef unsigned long long u64;
#define DI __device__ __forceinline__
#define LAS __attribute__((address_space(3)))
#define GAS __attribute__((address_space(1)))
typedef __amdgpu_buffer_rsrc_t rsrc_t;

constexpr unsigned WS_XT = 0;
constexpr unsigned WS_YT = 4u << 20;
constexpr unsigned WS_OF = 8u << 20;
constexpr unsigned WS_KVP = 12u << 20;
constexpr unsigned WS_ZP = 16u << 20;
constexpr unsigned WS_KVF = 17u << 20;
constexpr unsigned WS_ZF = WS_KVF + 131072;
constexpr unsigned WS_FL = 18u << 20;
constexpr unsigned FL_A1 = WS_FL, FL_Y = WS_FL + 8192, FL_O = WS_FL + 16384, FL_P = WS_FL + 24576, FL_R = WS_FL + 28672, FL_EPOCH = WS_FL + 32768;
constexpr unsigned WS_USED = 19u << 20;
constexpr int LDS_BYTES = 163840;

DI unsigned pk_bf16(float lo, float hi) { f32x2 p = {lo, hi}; return __builtin_bit_cast(unsigned, __builtin_convertvector(p, bfx2)); }
DI float bf_lo(unsigned u) { return __uint_as_float(u << 16); }
DI float bf_hi(unsigned u) { return __uint_as_float(u & 0xffff0000u); }
DI float rbf(float x) { return bf_lo(pk_bf16(x, 0.f)); }
DI f32x16 mfma32(bf16x8 a, bf16x8 b, f32x16 c) { return __builtin_amdgcn_mfma_f32_32x32x16_bf16(a, b, c, 0, 0, 0); }
DI f32x4 mfma16(bf16x8 a, bf16x8 b, f32x4 c) { return __builtin_amdgcn_mfma_f32_16x16x32_bf16(a, b, c, 0, 0, 0); }
DI s16x4 tr_read(const char* p) { return __builtin_amdgcn_ds_read_tr16_b64_v4i16((LAS s16x4*)p); }
DI bf16x8 cat8(s16x4 a, s16x4 b) { return __builtin_shufflevector(a, b, 0, 1, 2, 3, 4, 5, 6, 7); }
DI rsrc_t mk_rsrc(const void* p, unsigned bytes) { return __builtin_amdgcn_make_buffer_rsrc((void*)p, 0, bytes, 0x00020000); }
DI void blds16(rsrc_t r, unsigned voff, unsigned soff, void* l) { __builtin_amdgcn_raw_ptr_buffer_load_lds(r, (LAS void*)l, 16, voff, soff, 0, 0); }
#define LDAUX 16
#define STAUX 16
DI u32x4 ld16_sc1(rsrc_t r, unsigned voff, unsigned soff) { return __builtin_amdgcn_raw_buffer_load_b128(r, voff, soff, LDAUX); }
DI void st16_sc1(u32x4 v, rsrc_t r, unsigned voff, unsigned soff) { __builtin_amdgcn_raw_buffer_store_b128(v, r, voff + soff, 0, STAUX); }
#define WAIT_V(n) asm volatile("s_waitcnt vmcnt(%0)" ::"n"(n) : "memory")
#define WAIT_L0() asm volatile("s_waitcnt lgkmcnt(0)" ::: "memory")
#define BAR() do { WAIT_L0(); __builtin_amdgcn_s_barrier(); asm volatile("" ::: "memory"); } while (0)
#define SCHED0() __builtin_amdgcn_sched_barrier(0)
#define SGB(m, n) __builtin_amdgcn_sched_group_barrier(m, n, 0)
#define ST(kid, i) do { SCHED0(); u64 t_ = __builtin_amdgcn_s_memrealtime(); __builtin_amdgcn_s_waitcnt(0xC07F); if ((threadIdx.x & 63) == 0) ((u64*)(ws + (64u << 20)))[(((kid) * 256 + blockIdx.x) * 4 + ((threadIdx.x >> 6) & 3)) * 16 + (i)] = t_; SCHED0(); } while (0)
template <int N> using IC = std::integral_constant<int, N>;
using T_ = std::true_type; using F_ = std::false_type;

DI float xhalf(float v) { return __shfl_xor(v, 32); }
DI bf16x8 pack8(const f32x16& x, int s) {
  u32x4 p;
  p[0] = pk_bf16(x[8 * s + 0], x[8 * s + 1]); p[1] = pk_bf16(x[8 * s + 2], x[8 * s + 3]);
  p[2] = pk_bf16(x[8 * s + 4], x[8 * s + 5]); p[3] = pk_bf16(x[8 * s + 6], x[8 * s + 7]);
  return __builtin_bit_cast(bf16x8, p);
}
DI u64 flag_value(unsigned epoch) { const unsigned t = epoch + 1u; return ((u64)t << 32) | (u64)(t ^ 0x5bd1e995u); }
DI unsigned load_epoch(const char* ws) {
  return __builtin_amdgcn_readfirstlane(__hip_atomic_load((GAS const unsigned*)(ws + FL_EPOCH), __ATOMIC_RELAXED, __HIP_MEMORY_SCOPE_AGENT));
}
template <int YOUNGER = 0>
DI void publish(char* ws, unsigned flag_off, u64 fv, int lane) {
  WAIT_V(YOUNGER);
  if (lane == 0) __hip_atomic_store((GAS u64*)(ws + flag_off), fv, __ATOMIC_RELAXED, __HIP_MEMORY_SCOPE_AGENT);
}
DI void poll(const char* ws, unsigned flag_off, int n, u64 fv, int lane) {
  const GAS u64* p = (const GAS u64*)(ws + flag_off) + (lane < n ? lane : 0);
  for (unsigned spins = 0; spins < (1u << 20); ++spins) {
    const u64 v = __hip_atomic_load(p, __ATOMIC_RELAXED, __HIP_MEMORY_SCOPE_AGENT);
    if (__all(v == fv)) break;
#define PSLEEP 2
    __builtin_amdgcn_s_sleep(PSLEEP);
  }
  __builtin_amdgcn_fence(__ATOMIC_ACQUIRE, "wavefront");
  asm volatile("" ::: "memory");
}

DI void dual_softmax(const f32x16& pin, bf16x8* pos, bf16x8* neg) {
  float p[16];
#pragma unroll
  for (int i = 0; i < 8; ++i) { unsigned u = pk_bf16(pin[2 * i], pin[2 * i + 1]); p[2 * i] = bf_lo(u); p[2 * i + 1] = bf_hi(u); }
  float mx = p[0], mn = p[0];
#pragma unroll
  for (int i = 1; i < 16; ++i) { mx = fmaxf(mx, p[i]); mn = fminf(mn, p[i]); }
  mx = fmaxf(mx, xhalf(mx)); mn = fminf(mn, xhalf(mn));
  const float L2E = 1.4426950408889634f;
  float ep[16], en[16], sp = 0.f, sn = 0.f;
  const float mxl = mx * L2E, mnl = mn * L2E;
#pragma unroll
  for (int i = 0; i < 16; ++i) {
    ep[i] = __builtin_amdgcn_exp2f(fmaf(p[i], L2E, -mxl)); en[i] = __builtin_amdgcn_exp2f(fmaf(p[i], -L2E, mnl));
    sp += ep[i]; sn += en[i];
  }
  sp += xhalf(sp); sn += xhalf(sn);
  const float rp = __builtin_amdgcn_rcpf(sp), rn = __builtin_amdgcn_rcpf(sn);
#pragma unroll
  for (int s = 0; s < 2; ++s) {
    u32x4 a, b;
#pragma unroll
    for (int i = 0; i < 4; ++i) {
      a[i] = pk_bf16(ep[8 * s + 2 * i] * rp, ep[8 * s + 2 * i + 1] * rp);
      b[i] = pk_bf16(en[8 * s + 2 * i] * rn, en[8 * s + 2 * i + 1] * rn);
    }
    pos[s] = __builtin_bit_cast(bf16x8, a); neg[s] = __builtin_bit_cast(bf16x8, b);
  }
}

constexpr int G1_STAGE = 40960;
constexpr int L_WPHI = 122880;
constexpr int L_A1 = 126976;
constexpr int CV_WAVE = 20480;
constexpr int L_E1 = 81920;
constexpr int L_OUT = 131072;
constexpr int L_A3 = 147456;
DI int win_addr(int m0) {
  return ((m0 >> 3) << 6) + ((((m0 >> 1) & 3) ^ ((m0 >> 4) & 3)) << 4) + ((m0 & 1) << 3);
}
#define HBAR() do { __builtin_amdgcn_s_barrier(); asm volatile("" ::: "memory"); SCHED0(); } while (0)

DI void role_h(int b, int wave, char* sm, const u16* __restrict__ q, const u16* __restrict__ k, const u16* __restrict__ v,
               const u16* __restrict__ shk, const u16* __restrict__ hk, const u16* __restrict__ Dv, const u16* __restrict__ x,
               const u16* __restrict__ W, const u16* __restrict__ wo, char* ws, u16* __restrict__ out) {
  const int lane = threadIdx.x & 63;
  const int xcd = b & 7, a = xcd * 32 + (b >> 3), grp = a >> 4, idx = a & 15;
  const rsrc_t rw = mk_rsrc(ws, WS_USED);
  int hb_ = 0; (void)hb_;
  ST(1, 0);
  const unsigned epoch = __hip_atomic_load((GAS const unsigned*)(ws + FL_EPOCH), __ATOMIC_RELAXED, __HIP_MEMORY_SCOPE_AGENT);
  const int cgp = lane & 7, rg = lane >> 3;
  const int ch0 = grp * 64 + cgp * 8, r0 = idx * 128 + wave * 32 + rg * 4;
  const int ch = grp * 64 + idx * 4 + wave;
  u32x4 hv[4]; u32x2 hp[4];
  char* tl_ = sm + L_A1 + wave * 4096;
  const rsrc_t rt = (wave & 1) ? mk_rsrc(W, 1024 * 3072 * 2) : mk_rsrc(x, 2048 * 1024 * 2);
  unsigned tvo;
  {
    const int bi = b >> 3, l48 = lane < 48 ? lane : 0;
    tvo = (wave & 1) ? ((bi * 4 + l48 / 12) * 3072 + ((l48 % 12) >> 2) * 1024 + (l48 & 3) * 64 + (xcd >> 1) * 256) * 2
                     : (((xcd & 1) * 1024 + bi * 32 + (lane >> 1)) * 1024 + (lane & 1) * 64) * 2;
  }
  const unsigned tstep = (wave & 1) ? 128 * 3072 * 2 : 256;
#define TD 4
  auto touch = [&](int t) {
    if (!(t & 1) && t + TD < 16 && ((t >> 1) & 1) == (wave >> 1))
      __builtin_amdgcn_raw_ptr_buffer_load_lds(rt, (LAS void*)(sm + L_A3 + wave * 256), 4, tvo, ((t + TD) >> 1) * tstep, 0, 0);
  };
  if (TD >= 4) touch(-2);
  SCHED0();
  HBAR();
  u32x4 kk[7], vv[4], sk[4];
#pragma unroll
  for (int i = 0; i < 7; ++i) {
    const int r = r0 - 3 + i;
    kk[i] = *(const u32x4*)(k + (size_t)(r < 0 ? 0 : r) * 1024 + ch0);
  }
#pragma unroll
  for (int i = 0; i < 4; ++i) vv[i] = *(const u32x4*)(v + (size_t)(r0 + i) * 1024 + ch0);
#pragma unroll
  for (int i = 0; i < 4; ++i) sk[i] = *(const u32x4*)(shk + ch0 * 4 + i * 8);
  touch(0); HBAR(); HBAR(); touch(2); HBAR(); HBAR();
  {
    const u16* hrow = hk + (size_t)ch * 2048;
#pragma unroll
    for (int bb = 0; bb < 4; ++bb) {
      const int e0 = bb * 512 + lane * 8;
      hv[bb] = *(const u32x4*)(hrow + e0);
      hp[bb] = *(const u32x2*)(hrow + (e0 > 0 ? e0 - 4 : 0));
    }
  }
  const unsigned dvraw = Dv[ch];
  touch(4); HBAR(); HBAR();
#pragma unroll
  for (int i = 0; i < 3; ++i) if (r0 - 3 + i < 0) kk[i] = u32x4{0u, 0u, 0u, 0u};
#pragma unroll
  for (int e2 = 0; e2 < 4; ++e2) {
    f32x2 w[4];
    w[0] = f32x2{bf_lo(sk[e2][0]), bf_lo(sk[e2][2])}; w[1] = f32x2{bf_hi(sk[e2][0]), bf_hi(sk[e2][2])};
    w[2] = f32x2{bf_lo(sk[e2][1]), bf_lo(sk[e2][3])}; w[3] = f32x2{bf_hi(sk[e2][1]), bf_hi(sk[e2][3])};
    f32x2 kf[7], o[4];
#pragma unroll
    for (int i = 0; i < 7; ++i) kf[i] = f32x2{bf_lo(kk[i][e2]), bf_hi(kk[i][e2])};
#pragma unroll
    for (int i = 0; i < 4; ++i) {
      f32x2 acc = kf[i] * w[3];
      acc = __builtin_elementwise_fma(kf[i + 1], w[2], acc);
      acc = __builtin_elementwise_fma(kf[i + 2], w[1], acc);
      acc = __builtin_elementwise_fma(kf[i + 3], w[0], acc);
      const unsigned ks = pk_bf16(acc[0], acc[1]);
      o[i] = f32x2{bf_lo(ks), bf_hi(ks)} * f32x2{bf_lo(vv[i][e2]), bf_hi(vv[i][e2])};
    }
    u32x2 wa = {pk_bf16(o[0][0], o[1][0]), pk_bf16(o[2][0], o[3][0])}, wb = {pk_bf16(o[0][1], o[1][1]), pk_bf16(o[2][1], o[3][1])};
    *(u32x2*)(tl_ + (cgp * 8 + 2 * e2) * 64 + ((rg ^ (cgp & 6)) << 3)) = wa;
    *(u32x2*)(tl_ + (cgp * 8 + 2 * e2 + 1) * 64 + ((rg ^ (cgp & 6)) << 3)) = wb;
    if (e2 & 1) { touch(6 + (e2 >> 1)); HBAR(); }
  }
#pragma unroll
  for (int it = 0; it < 4; ++it) {
    const int c = it * 16 + (lane >> 2), ck = lane & 3;
    const u32x4 d = *(const u32x4*)(tl_ + c * 64 + ((ck ^ ((c >> 4) & 3)) << 4));
    st16_sc1(d, rw, (grp * 64 + c) * 4096 + (idx * 128 + wave * 32 + ck * 8) * 2, WS_XT);
  }
  ST(1, 1);
  touch(8); HBAR(); HBAR();
  const u64 fv = flag_value(__builtin_amdgcn_readfirstlane(epoch));
  publish(ws, FL_A1 + (grp * 64 + idx * 4 + wave) * 8, fv, lane);
  ST(1, 2);
  touch(10); HBAR(); HBAR(); HBAR(); HBAR();
  poll(ws, FL_A1 + grp * 512, 64, fv, lane);
  u32x4 xv[4];
#pragma unroll
  for (int bb = 0; bb < 4; ++bb) xv[bb] = ld16_sc1(rw, ch * 4096 + bb * 1024 + lane * 16, WS_XT);
  ST(1, 3);
  HBAR(); HBAR();
  ST(1, 4);
  char* wl = sm + wave * CV_WAVE;
  char* xl = wl + 16384;
  const float dcoef = bf_lo(dvraw);
  if (lane == 0) { hp[0][0] = 0u; hp[0][1] = 0u; }
#pragma unroll
  for (int bb = 0; bb < 4; ++bb) {
    *(u32x4*)(xl + (lane & 1) * 2048 + (bb * 32 + (lane >> 1)) * 16) = xv[bb];
    const unsigned dm2 = hp[bb][0], dm1 = hp[bb][1], d0 = hv[bb][0], d1 = hv[bb][1], d2 = hv[bb][2], d3 = hv[bb][3];
    unsigned R[10];
    R[0] = __builtin_amdgcn_perm(dm2, dm1, 0x07060100u);
    R[1] = __builtin_amdgcn_alignbit(dm1, dm1, 16);
    R[2] = __builtin_amdgcn_perm(dm1, d0, 0x07060100u);
    R[3] = __builtin_amdgcn_alignbit(d0, d0, 16);
    R[4] = __builtin_amdgcn_perm(d0, d1, 0x07060100u);
    R[5] = __builtin_amdgcn_alignbit(d1, d1, 16);
    R[6] = __builtin_amdgcn_perm(d1, d2, 0x07060100u);
    R[7] = __builtin_amdgcn_alignbit(d2, d2, 16);
    R[8] = __builtin_amdgcn_perm(d2, d3, 0x07060100u);
    R[9] = __builtin_amdgcn_alignbit(d3, d3, 16);
    const int i = bb * 64 + lane, sw = (i >> 1) & 3;
#pragma unroll
    for (int r = 0; r < 4; ++r) {
      u32x4 w = {R[2 * r + 2], R[2 * r], R[2 * r + 3], R[2 * r + 1]};
      *(u32x4*)(wl + i * 64 + ((r ^ sw) << 4)) = w;
    }
  }
  ST(1, 5);
  const int ca = lane & 15, g = lane >> 4;
  const int off = (g & 1) * 16 + (g >> 1) * 8;
  const int cb = 2 * ca + 1 - (off >> 3);
  const int xb0 = 16384 + (cb & 1) * 2048 + ((cb >> 1) << 4);
  f32x4 acc[8];
#pragma unroll
  for (int c = 0; c < 8; ++c) acc[c] = f32x4{0.f, 0.f, 0.f, 0.f};
  bf16x8 A[2][8], B[2][8];
  const bf16x8 zero8 = {0, 0, 0, 0, 0, 0, 0, 0};
  const int wbase = wave * CV_WAVE;
  auto ld = [&](int set, int t0, auto FIRST) {
    const int m0 = 32 * t0 + ca + off - 8;
    const char* pb = wl + xb0 - t0 * 32;
    if constexpr (FIRST.value) {
      const s16x4 z4 = {0, 0, 0, 0};
      s16x4 a0 = *(const s16x4*)(wl + win_addr(m0 < 0 ? 0 : m0)), a1 = *(const s16x4*)(wl + win_addr(m0 - 4 < 0 ? 0 : m0 - 4));
      A[set][0] = cat8(m0 < 0 ? z4 : a0, m0 - 4 < 0 ? z4 : a1);
    }
    const int pa0 = wbase + win_addr(m0 + 256) - 2048, pa1 = wbase + win_addr(m0 + 252) - 2048;
#pragma unroll
    for (int i = 0; i < 8; ++i) {
      B[set][7 - i] = *(const bf16x8*)(pb + (7 - i) * 256);
      if (!(FIRST.value && i == 0)) {
        int q0 = pa0 + i * 2048, q1 = pa1 + i * 2048;
        asm volatile("" : "+v"(q0), "+v"(q1));
        A[set][i] = cat8(*(const s16x4*)(sm + q0), *(const s16x4*)(sm + q1));
      }
    }
  };
  auto mm = [&](int set, int t0) {
    const bf16x8 b0 = (cb - 4 * t0 < 0) ? zero8 : B[set][0];
#pragma unroll
    for (int t1 = 0; t1 < 8; ++t1)
#pragma unroll
      for (int i = 7 - t1; i >= 0; --i) acc[t1 + i] = mfma16(A[set][t1], i == 0 ? b0 : B[set][i], acc[t1 + i]);
  };
  ld(0, 0, T_{});
  SCHED0();
#define CV_SCHED() do { _Pragma("unroll") for (int z = 0; z < 24; ++z) { SGB(0x8, 1); SGB(0x100, 1); } SGB(0x8, 12); SCHED0(); \
    asm volatile("" : "+v"(acc[0]), "+v"(acc[1]), "+v"(acc[2]), "+v"(acc[3]), "+v"(acc[4]), "+v"(acc[5]), "+v"(acc[6]), "+v"(acc[7])); } while (0)
#pragma unroll 1
  for (int t0 = 0; t0 < 6; t0 += 2) {
    ld(1, t0 + 1, F_{}); mm(0, t0); CV_SCHED();
    ld(0, t0 + 2, F_{}); mm(1, t0 + 1); CV_SCHED();
  }
  ld(1, 7, F_{}); mm(0, 6); CV_SCHED();
  mm(1, 7);
  ST(1, 6);
  u32x4 qq[4];
#pragma unroll
  for (int i = 0; i < 4; ++i) qq[i] = *(const u32x4*)(q + (size_t)(r0 + i) * 1024 + ch0);
  char* yl = sm + L_A3 + wave * 4096;
  {
    const int xe = 16384 + (g >> 1) * 2048 + (ca << 4) + (g & 1) * 8;
#pragma unroll
    for (int c = 0; c < 8; ++c) {
      const u32x2 kv = *(const u32x2*)(wl + xe + c * 256);
      const unsigned y01 = pk_bf16(acc[c][0], acc[c][1]), y23 = pk_bf16(acc[c][2], acc[c][3]);
      const unsigned t01 = pk_bf16(bf_lo(kv[0]) * dcoef, bf_hi(kv[0]) * dcoef), t23 = pk_bf16(bf_lo(kv[1]) * dcoef, bf_hi(kv[1]) * dcoef);
      u32x2 o = {pk_bf16(bf_lo(y01) + bf_lo(t01), bf_hi(y01) + bf_hi(t01)), pk_bf16(bf_lo(y23) + bf_lo(t23), bf_hi(y23) + bf_hi(t23))};
      *(u32x2*)(yl + c * 512 + ca * 32 + g * 8) = o;
    }
#pragma unroll
    for (int it = 0; it < 4; ++it) st16_sc1(*(const u32x4*)(yl + it * 1024 + lane * 16), rw, ch * 4096 + it * 1024 + lane * 16, WS_YT);
  }
  WAIT_L0();
  {
    const rsrc_t ro = mk_rsrc(wo, 1024 * 1024 * 2);
    const int j = (xcd >> 1) * 4 + (b >> 6);
    const unsigned wosrc = ((wave * 160 + ((lane >> 2) & 7)) * 1024 + j * 64 + (lane >> 5) * 32 + (lane & 3) * 8) * 2;
#pragma unroll
    for (int p = 0; p < 20; ++p) blds16(ro, wosrc, p * 16384, wl + p * 1024);
  }
  publish<20>(ws, FL_Y + (grp * 64 + idx * 4 + wave) * 8, fv, lane);
  ST(1, 7);
  WAIT_V(0);
  HBAR();
  poll(ws, FL_Y + grp * 512, 64, fv, lane);
  ST(1, 8);
  {
    u32x4 yy[4];
#pragma unroll
    for (int it = 0; it < 4; ++it) {
      const int c = it * 16 + (lane >> 2), ck = lane & 3;
      yy[it] = ld16_sc1(rw, (grp * 64 + c) * 4096 + (idx * 128 + wave * 32 + ck * 8) * 2, WS_YT);
    }
#pragma unroll
    for (int it = 0; it < 4; ++it) {
      const int c = it * 16 + (lane >> 2), ck = lane & 3;
      *(u32x4*)(yl + c * 64 + ((ck ^ ((c >> 4) & 3)) << 4)) = yy[it];
    }
    u32x2 y[8];
#pragma unroll
    for (int e = 0; e < 8; ++e) y[e] = *(const u32x2*)(yl + (cgp * 8 + e) * 64 + ((rg ^ (cgp & 6)) << 3));
#pragma unroll
    for (int i = 0; i < 4; ++i) {
      u32x4 o;
#pragma unroll
      for (int e2 = 0; e2 < 4; ++e2) {
        const unsigned ya = y[2 * e2][i >> 1], yb = y[2 * e2 + 1][i >> 1];
        const float fa = (i & 1) ? bf_hi(ya) : bf_lo(ya), fb = (i & 1) ? bf_hi(yb) : bf_lo(yb);
        o[e2] = pk_bf16(bf_lo(qq[i][e2]) * fa, bf_hi(qq[i][e2]) * fb);
      }
      *(u32x4*)(out + (size_t)(r0 + i) * 2048 + ch0) = o;
    }
  }
  ST(1, 9);
}

DI void role_g(int bid, int wave, char* sm, const u16* __restrict__ x, const u16* __restrict__ W, const u16* __restrict__ wphi,
               const u16* __restrict__ wo, char* ws, u16* __restrict__ out) {
  int lane = threadIdx.x & 63;
  const int xcd = bid & 7, idx = bid >> 3;
  const int rb = (xcd & 1) * 8 + (idx & 7), j = (xcd >> 1) * 4 + (idx >> 3);
  const int wm = wave & 1, wn = wave >> 1;
  int h = lane >> 5, l31 = lane & 31;
  int blk = (lane >> 4) & 1, q4 = (lane & 15) >> 2, p4 = lane & 3;
  int troff = q4 * 64 + (2 * blk + (p4 >> 1)) * 16 + (p4 & 1) * 8;
  const int head = 2 * j + wn;
#define RELANE() do { asm volatile("" : "+v"(lane)); h = lane >> 5; l31 = lane & 31; blk = (lane >> 4) & 1; q4 = (lane & 15) >> 2; p4 = lane & 3; \
    troff = q4 * 64 + (2 * blk + (p4 >> 1)) * 16 + (p4 & 1) * 8; SCHED0(); } while (0)
  const rsrc_t rx = mk_rsrc(x, 2048 * 1024 * 2), rW = mk_rsrc(W, 1024 * 3072 * 2), rp = mk_rsrc(wphi, 65536), rw = mk_rsrc(ws, WS_USED);
  ST(0, 0);
  f32x16 zero;
#pragma unroll
  for (int i = 0; i < 16; ++i) zero[i] = 0.f;
  volatile LAS unsigned* hs = (volatile LAS unsigned*)(sm + L_A1 + 16384);
  if (wm == 0 && lane == 0) hs[wn] = 0u;
  f32x16 acc[2][3];
  {
    unsigned xs[4], wsrc[6];
#pragma unroll
    for (int i = 0; i < 4; ++i) {
      const int p = wave * 4 + i, row = p * 8 + (lane >> 3), c = (lane & 7) ^ ((row >> 1) & 7);
      xs[i] = ((rb * 128 + row) * 1024 + c * 8) * 2;
    }
#pragma unroll
    for (int i = 0; i < 6; ++i) {
      const int p = wave * 6 + i, kg = p / 3, seg = p % 3;
      wsrc[i] = ((kg * 8 + ((lane >> 2) & 7)) * 3072 + seg * 1024 + j * 64 + (lane >> 5) * 32 + (lane & 3) * 8) * 2;
    }
    auto piece = [&](int t, int buf, int i) {
      char* bb = sm + buf * G1_STAGE;
      if (i < 4) blds16(rx, xs[i], t * 128, bb + (wave * 4 + i) * 1024);
      else blds16(rW, wsrc[i - 4], t * (64 * 3072 * 2), bb + 16384 + (wave * 6 + i - 4) * 1024);
    };
#pragma unroll
    for (int i = 0; i < 10; ++i) piece(0, 0, i);
    blds16(rp, (j * 2048 + wave * 512 + lane * 8) * 2, 0, sm + L_WPHI + wave * 1024);
#pragma unroll
    for (int i = 0; i < 10; ++i) piece(1, 1, i);
#pragma unroll
    for (int i = 0; i < 4; ++i) piece(2, 2, i);
    int xoff[4];
    {
      const int row = wm * 64 + l31, sw = (row >> 1) & 7;
#pragma unroll
      for (int s = 0; s < 4; ++s) xoff[s] = row * 128 + (((2 * s + h) ^ sw) << 4);
    }
    const int woff = 16384 + h * 3072 + wn * 512 + troff;
#pragma unroll
    for (int a2 = 0; a2 < 2; ++a2)
#pragma unroll
      for (int b2 = 0; b2 < 3; ++b2) acc[a2][b2] = zero;
    bf16x8 xf[2][2], wf[2][3];
    auto rd = [&](int set, const char* bb, int s) {
      xf[set][0] = *(const bf16x8*)(bb + xoff[s]);
#pragma unroll
      for (int gq = 0; gq < 3; ++gq) wf[set][gq] = cat8(tr_read(bb + woff + s * 6144 + gq * 1024), tr_read(bb + woff + s * 6144 + gq * 1024 + 256));
      xf[set][1] = *(const bf16x8*)(bb + xoff[s] + 4096);
    };
    auto mm = [&](int set) {
#pragma unroll
      for (int ts = 0; ts < 2; ++ts) {
        acc[ts][0] = mfma32(wf[set][0], xf[set][ts], acc[ts][0]);
        acc[ts][1] = mfma32(wf[set][1], xf[set][ts], acc[ts][1]);
        acc[ts][2] = mfma32(xf[set][ts], wf[set][2], acc[ts][2]);
      }
    };
    auto pin = [&]() { asm volatile("" : "+v"(acc[0][0]), "+v"(acc[0][1]), "+v"(acc[0][2]), "+v"(acc[1][0]), "+v"(acc[1][1]), "+v"(acc[1][2])); };
#define G1_SCHED(NV) do { SGB(0x100, 3); SGB(0x8, 1); SGB(0x100, 2); SGB(0x8, 1); SGB(0x100, 3); SGB(0x8, 1); \
    if ((NV) > 0) SGB(0x10, 1); SGB(0x8, 1); if ((NV) > 1) SGB(0x10, 1); SGB(0x8, 1); if ((NV) > 2) SGB(0x10, 1); SGB(0x8, 1); if ((NV) > 3) SGB(0x10, 1); SCHED0(); } while (0)
    ST(0, 1);
    WAIT_V(15); BAR();
    ST(0, 2);
    rd(0, sm, 0);
    SCHED0();
    int cur = 0, nxt = 1, nx2 = 2;
    auto step = [&](int t, auto BC, auto AA, auto WN, auto RN) {
      const char* bb = sm + cur * G1_STAGE;
      rd(1, bb, 1);
      if constexpr (BC.value) { piece(t + 2, nx2, 4); piece(t + 2, nx2, 5); piece(t + 2, nx2, 6); }
      mm(0); G1_SCHED(BC.value ? 3 : 0); pin();
      rd(0, bb, 2);
      if constexpr (BC.value) { piece(t + 2, nx2, 7); piece(t + 2, nx2, 8); piece(t + 2, nx2, 9); }
      mm(1); G1_SCHED(BC.value ? 3 : 0); pin();
      rd(1, bb, 3); mm(0); G1_SCHED(0); pin();
      WAIT_V(WN.value);
      BAR();
      SCHED0();
      if constexpr (RN.value) rd(0, sm + nxt * G1_STAGE, 0);
      if constexpr (AA.value) { piece(t + 3, cur, 0); piece(t + 3, cur, 1); piece(t + 3, cur, 2); piece(t + 3, cur, 3); }
      mm(1); G1_SCHED(AA.value ? 4 : 0); pin();
      const int o = cur; cur = nxt; nxt = nx2; nx2 = o;
    };
#pragma unroll 1
    for (int t = 0; t < 13; ++t) step(t, T_{}, T_{}, IC<10>{}, T_{});
    step(13, T_{}, F_{}, IC<10>{}, T_{});
    step(14, F_{}, F_{}, IC<0>{}, T_{});
    step(15, F_{}, F_{}, IC<0>{}, F_{});
  }
  ST(0, 3);
  RELANE();
  const unsigned epoch = __hip_atomic_load((GAS const unsigned*)(ws + FL_EPOCH), __ATOMIC_RELAXED, __HIP_MEMORY_SCOPE_AGENT);
  bf16x8 wp[2];
  {
    const char* wb = sm + L_WPHI + wn * 2048 + h * 256 + troff;
#pragma unroll
    for (int s = 0; s < 2; ++s) wp[s] = cat8(tr_read(wb + s * 1024), tr_read(wb + s * 1024 + 512));
  }
  const rsrc_t ro = mk_rsrc(wo, 1024 * 1024 * 2);
  u64 fv;
  {
    char* img = sm + L_E1 + wave * 8192;
#pragma unroll
    for (int ts = 0; ts < 2; ++ts) {
      f32x16 pk = mfma32(wp[0], pack8(acc[ts][1], 0), zero);
      pk = mfma32(wp[1], pack8(acc[ts][1], 1), pk);
      bf16x8 pos[2], neg[2];
      dual_softmax(pk, pos, neg);
      const int row = ts * 32 + l31, sw = (row >> 1) & 7;
#pragma unroll
      for (int gq = 0; gq < 4; ++gq) {
        const u32x4 pp = __builtin_bit_cast(u32x4, pos[gq >> 1]), nn = __builtin_bit_cast(u32x4, neg[gq >> 1]);
        u32x2 aa = {pp[(gq & 1) * 2], pp[(gq & 1) * 2 + 1]}, bb = {nn[(gq & 1) * 2], nn[(gq & 1) * 2 + 1]};
        const int o = row * 64 + (((2 * gq + h) ^ sw) << 3);
        *(u32x2*)(img + o) = aa;
        *(u32x2*)(img + 4096 + o) = bb;
      }
    }
    f32x16 kvacc[2], zacc[2];
    kvacc[0] = zero; kvacc[1] = zero; zacc[0] = zero; zacc[1] = zero;
    bf16x8 ones;
#pragma unroll
    for (int i = 0; i < 8; ++i) ones[i] = (short)0x3f80;
#pragma unroll
    for (int ts = 0; ts < 2; ++ts)
#pragma unroll
      for (int s = 0; s < 2; ++s) {
        const bf16x8 vb = pack8(acc[ts][2], s);
        const int r0 = ts * 32 + 16 * s + 4 * h + q4, r1 = r0 + 8;
        const int o0 = r0 * 64 + (((4 * blk + p4) ^ ((r0 >> 1) & 7)) << 3), o1 = r1 * 64 + (((4 * blk + p4) ^ ((r1 >> 1) & 7)) << 3);
#pragma unroll
        for (int tl = 0; tl < 2; ++tl) {
          const bf16x8 pa = cat8(tr_read(img + tl * 4096 + o0), tr_read(img + tl * 4096 + o1));
          kvacc[tl] = mfma32(pa, vb, kvacc[tl]);
          zacc[tl] = mfma32(pa, ones, zacc[tl]);
        }
      }
    fv = flag_value(__builtin_amdgcn_readfirstlane(epoch));
    asm volatile("" ::"s"(fv));
    char* red = sm + L_E1 + (wn * 2 + 1) * 8192;
    char* zred = sm + L_E1 + 32768 + wn * 256;
    if (wm == 1) {
#pragma unroll
      for (int tl = 0; tl < 2; ++tl)
#pragma unroll
        for (int qd = 0; qd < 4; ++qd) {
          f32x4 t = {kvacc[tl][4 * qd], kvacc[tl][4 * qd + 1], kvacc[tl][4 * qd + 2], kvacc[tl][4 * qd + 3]};
          *(f32x4*)(red + (tl * 4 + qd) * 1024 + lane * 16) = t;
        }
      if (l31 == 0) {
#pragma unroll
        for (int tl = 0; tl < 2; ++tl)
#pragma unroll
          for (int qd = 0; qd < 4; ++qd) {
            f32x4 z = {zacc[tl][4 * qd], zacc[tl][4 * qd + 1], zacc[tl][4 * qd + 2], zacc[tl][4 * qd + 3]};
            *(f32x4*)(zred + ((tl * 2 + h) * 4 + qd) * 16) = z;
          }
      }
      WAIT_L0();
      if (lane == 0) hs[wn] = 1u;
    } else {
#pragma unroll 1
      for (unsigned spins = 0; spins < (1u << 24) && hs[wn] == 0u; ++spins) __builtin_amdgcn_s_sleep(1);
      asm volatile("" ::: "memory");
#pragma unroll
      for (int tl = 0; tl < 2; ++tl)
#pragma unroll
        for (int qd = 0; qd < 4; ++qd) {
          f32x4 t = *(const f32x4*)(red + (tl * 4 + qd) * 1024 + lane * 16);
#pragma unroll
          for (int i = 0; i < 4; ++i) t[i] += kvacc[tl][4 * qd + i];
          st16_sc1(__builtin_bit_cast(u32x4, t), rw, ((rb * 32 + head) * 8 + tl * 4 + qd) * 1024 + lane * 16, WS_KVP);
        }
      if (l31 == 0) {
#pragma unroll
        for (int tl = 0; tl < 2; ++tl)
#pragma unroll
          for (int qd = 0; qd < 4; ++qd) {
            f32x4 z = *(const f32x4*)(zred + ((tl * 2 + h) * 4 + qd) * 16);
#pragma unroll
            for (int i = 0; i < 4; ++i) z[i] += zacc[tl][4 * qd + i];
            st16_sc1(__builtin_bit_cast(u32x4, z), rw, (rb * 32 + head) * 256 + ((tl * 2 + h) * 4 + qd) * 16, WS_ZP);
          }
      }
    }
  }
  ST(0, 4);
  bf16x8 ph[2][4];
  {
    f32x16 pq = mfma32(wp[0], pack8(acc[0][0], 0), zero);
    pq = mfma32(wp[1], pack8(acc[0][0], 1), pq);
    dual_softmax(pq, ph[0], ph[0] + 2);
  }
  SCHED0();
  if (wm == 0) publish(ws, FL_P + (head * 16 + rb) * 8, fv, lane);
  {
    f32x16 pq = mfma32(wp[0], pack8(acc[1][0], 0), zero);
    pq = mfma32(wp[1], pack8(acc[1][0], 1), pq);
    dual_softmax(pq, ph[1], ph[1] + 2);
  }
  ST(0, 5);
  RELANE();
  if (wave == 1) {
    const int rhead = 2 * j + (rb >> 3), row = rb & 7;
    poll(ws, FL_P + rhead * 128, 16, fv, lane);
    ST(0, 6);
    u32x4 e[16];
#pragma unroll
    for (int r = 0; r < 16; ++r) e[r] = ld16_sc1(rw, (rhead * 8 + row) * 1024 + lane * 16, WS_KVP + r * 262144);
    float ze[16];
    if (row == 0) {
#pragma unroll
      for (int r = 0; r < 16; ++r) ze[r] = __uint_as_float(__builtin_amdgcn_raw_buffer_load_b32(rw, rhead * 256 + lane * 4, WS_ZP + r * 8192, 16));
    }
    f32x4 s = {0.f, 0.f, 0.f, 0.f};
#pragma unroll
    for (int r = 0; r < 16; ++r) s += __builtin_bit_cast(f32x4, e[r]);
    u32x2 o = {pk_bf16(s[0], s[1]), pk_bf16(s[2], s[3])};
    __builtin_amdgcn_raw_buffer_store_b64(o, rw, (rhead * 4 + (row >> 1)) * 1024 + lane * 16 + (row & 1) * 8, WS_KVF, 16);
    if (row == 0) {
      float zs = 0.f;
#pragma unroll
      for (int r = 0; r < 16; ++r) zs += ze[r];
      const float zn = __shfl_down(zs, 1);
      if ((lane & 1) == 0) __builtin_amdgcn_raw_buffer_store_b32(pk_bf16(zs, zn), rw, rhead * 128 + lane * 2, WS_ZF, 16);
    }
    publish(ws, FL_R + (j * 16 + rb) * 8, fv, lane);
    ST(0, 7);
  }
  {
    poll(ws, FL_R + (j * 16 + wn * 8) * 8, 8, fv, lane);
    ST(0, 8);
    bf16x8 kvf[4], zf[4];
#pragma unroll
    for (int i = 0; i < 4; ++i) {
      kvf[i] = __builtin_bit_cast(bf16x8, ld16_sc1(rw, (head * 4 + i) * 1024 + lane * 16, WS_KVF));
      zf[i] = __builtin_bit_cast(bf16x8, ld16_sc1(rw, head * 128 + ((i >> 1) * 2 + h) * 32 + (i & 1) * 16, WS_ZF));
    }
#pragma unroll
    for (int ts = 0; ts < 2; ++ts) {
      f32x16 num = zero, den = zero;
#pragma unroll
      for (int i = 0; i < 4; ++i) { num = mfma32(kvf[i], ph[ts][i], num); den = mfma32(zf[i], ph[ts][i], den); }
      const float dn = rbf(rbf(den[0]) + 1e-6f);
      const float rdn = 1.0f / dn;
      f32x16 o;
#pragma unroll
      for (int i = 0; i < 8; ++i) {
        const unsigned u = pk_bf16(num[2 * i], num[2 * i + 1]);
        o[2 * i] = bf_lo(u) * rdn; o[2 * i + 1] = bf_hi(u) * rdn;
      }
      const unsigned dst = (((rb * 4 + wm * 2 + ts) * 32 + head) * 2) * 1024 + lane * 16;
      st16_sc1(__builtin_bit_cast(u32x4, pack8(o, 0)), rw, dst, WS_OF);
      st16_sc1(__builtin_bit_cast(u32x4, pack8(o, 1)), rw, dst + 1024, WS_OF);
    }
    poll(ws, FL_R + (j * 16 + (wn ^ 1) * 8) * 8, 8, fv, lane);
    publish(ws, FL_O + (rb * 64 + wm * 32 + j * 2 + wn) * 8, fv, lane);
    {
      const unsigned wosrc = (((80 + wave * 12) * 8 + ((lane >> 2) & 7)) * 1024 + j * 64 + (lane >> 5) * 32 + (lane & 3) * 8) * 2;
#pragma unroll
      for (int p = 0; p < 12; ++p) blds16(ro, wosrc, p * 16384, sm + (80 + wave * 12 + p) * 1024);
    }
  }
  ST(0, 9);
  {
    poll(ws, FL_O + (rb * 64 + (wave >> 1) * 32) * 8, 32, fv, lane);
    ST(0, 10);
    const unsigned osrc = (rb * 4 + wave) * 65536 + lane * 16;
    bf16x8 of[32];
#pragma unroll
    for (int i = 0; i < 32; ++i) of[i] = __builtin_bit_cast(bf16x8, ld16_sc1(rw, osrc + i * 1024, WS_OF));
    f32x16 oa[2];
    oa[0] = zero; oa[1] = zero;
    bf16x8 wf[2][2];
    const char* wb = sm + h * 256 + troff;
    auto rd = [&](int set, const char* bb) {
      wf[set][0] = cat8(tr_read(bb), tr_read(bb + 1024));
      wf[set][1] = cat8(tr_read(bb + 512), tr_read(bb + 1536));
    };
    BAR();
    ST(0, 11);
    rd(0, wb);
    SCHED0();
    auto chunk = [&](int c, auto RELOAD, auto LAST) {
      const char* bb = wb + c * 65536;
#pragma unroll
      for (int i = 0; i < 32; ++i) {
        if (!(i == 31 && LAST.value)) rd((i + 1) & 1, bb + (i + 1) * 2048);
        oa[0] = mfma32(wf[i & 1][0], of[i], oa[0]);
        oa[1] = mfma32(wf[i & 1][1], of[i], oa[1]);
        if constexpr (RELOAD.value) of[i] = __builtin_bit_cast(bf16x8, ld16_sc1(rw, osrc + i * 1024, WS_OF + 32768));
        SGB(0x100, 2); SGB(0x8, 1); SGB(0x100, 2); SGB(0x8, 1); SGB(0x10, 1); SCHED0();
        asm volatile("" : "+v"(oa[0]), "+v"(oa[1]));
      }
    };
    chunk(0, T_{}, F_{});
    chunk(1, F_{}, T_{});
    ST(0, 12);
    RELANE();
    char* ot = sm + L_OUT + wave * 4096;
#pragma unroll
    for (int gq = 0; gq < 2; ++gq)
#pragma unroll
      for (int qd = 0; qd < 4; ++qd) {
        u32x2 w = {pk_bf16(oa[gq][4 * qd], oa[gq][4 * qd + 1]), pk_bf16(oa[gq][4 * qd + 2], oa[gq][4 * qd + 3])};
        *(u32x2*)(ot + l31 * 128 + (((4 * gq + qd) ^ (l31 & 7)) << 4) + h * 8) = w;
      }
#pragma unroll
    for (int it = 0; it < 4; ++it) {
      const int row = it * 8 + (lane >> 3), c = lane & 7;
      const u32x4 d = *(const u32x4*)(ot + row * 128 + ((c ^ (row & 7)) << 4));
      *(u32x4*)(out + (size_t)(rb * 128 + wave * 32 + row) * 2048 + 1024 + j * 64 + c * 8) = d;
    }
  }
  if (bid == 0 && threadIdx.x == 0) __hip_atomic_store((GAS unsigned*)(ws + FL_EPOCH), epoch + 1u, __ATOMIC_RELAXED, __HIP_MEMORY_SCOPE_AGENT);
  ST(0, 13);
}

__global__ __launch_bounds__(512, 2) void fused_kernel(const u16* q, const u16* k, const u16* v, const u16* shk, const u16* hk, const u16* Dv,
                                                        const u16* x, const u16* W, const u16* wphi, const u16* wo, char* ws, u16* out) {
  extern __shared__ __attribute__((aligned(16))) char sm[];
  const int wave = __builtin_amdgcn_readfirstlane(threadIdx.x >> 6);
  if (wave < 4) role_g(blockIdx.x, wave, sm, x, W, wphi, wo, ws, out);
  else role_h(blockIdx.x, wave - 4, sm, q, k, v, shk, hk, Dv, x, W, wo, ws, out);
}

__global__ void k_spin(char* ws) {
  const u64* st = (const u64*)(ws + (64u << 20));
  u64 amn = ~0ull, amx = 0, bmn = ~0ull, bmx = 0;
  for (int i = threadIdx.x; i < 1024; i += 64) {
    if (W0ONLY && (i & 3)) continue;
    u64 a = st[(KA * 1024 + i) * 16 + IA], b = st[(KB * 1024 + i) * 16 + IB];
    amn = a < amn ? a : amn; amx = a > amx ? a : amx; bmn = b < bmn ? b : bmn; bmx = b > bmx ? b : bmx;
  }
  for (int o = 32; o; o >>= 1) {
    u64 t;
    t = __shfl_xor(amn, o); amn = t < amn ? t : amn; t = __shfl_xor(amx, o); amx = t > amx ? t : amx;
    t = __shfl_xor(bmn, o); bmn = t < bmn ? t : bmn; t = __shfl_xor(bmx, o); bmx = t > bmx ? t : bmx;
  }
  long long T = (long long)((BMAX ? bmx : bmn) - (AMIN ? amn : amx));
  if (T < 0) T = 0;
  if (T > 10000) T = 10000;
  u64 r0 = __builtin_amdgcn_s_memrealtime();
  while (__builtin_amdgcn_s_memrealtime() - r0 < (u64)T * 10) __builtin_amdgcn_s_sleep(1);
}

extern "C" void kernel_launch(void* const* d_in, const int* in_sizes, int n_in, void* d_out, int out_size, void* d_ws, size_t ws_size,
                              hipStream_t stream) {
  (void)hipFuncSetAttribute((const void*)fused_kernel, hipFuncAttributeMaxDynamicSharedMemorySize, LDS_BYTES);
  fused_kernel<<<256, 512, LDS_BYTES, stream>>>((const u16*)d_in[0], (const u16*)d_in[1], (const u16*)d_in[2], (const u16*)d_in[3],
                                                (const u16*)d_in[4], (const u16*)d_in[5], (const u16*)d_in[6], (const u16*)d_in[7],
                                                (const u16*)d_in[8], (const u16*)d_in[9], (char*)d_ws, (u16*)d_out);
  k_spin<<<1, 64, 0, stream>>>((char*)d_ws);
}
```

```cpp
#include <hip/hip_runtime.h>
#include <stdint.h>
#include <type_traits>

typedef short bf16x8 __attribute__((ext_vector_type(8)));
typedef short s16x4 __attribute__((ext_vector_type(4)));
typedef float f32x4 __attribute__((ext_vector_type(4)));
typedef float f32x2 __attribute__((ext_vector_type(2)));
typedef float f32x16 __attribute__((ext_vector_type(16)));
typedef unsigned u32x4 __attribute__((ext_vector_type(4)));
typedef unsigned u32x2 __attribute__((ext_vector_type(2)));
typedef __bf16 bfx2 __attribute__((ext_vector_type(2)));
typedef unsigned short u16;
typedef unsigned long long u64;
#define DI __device__ __forceinline__
#define LAS __attribute__((address_space(3)))
#define GAS __attribute__((address_space(1)))
typedef __amdgpu_buffer_rsrc_t rsrc_t;

constexpr unsigned WS_XT = 0;
constexpr unsigned WS_YT = 4u << 20;
constexpr unsigned WS_OF = 8u << 20;
constexpr unsigned WS_KVP = 12u << 20;
constexpr unsigned WS_ZP = 16u << 20;
constexpr unsigned WS_FL = 18u << 20;
constexpr unsigned FL_A1 = WS_FL, FL_Y = WS_FL + 8192, FL_O = WS_FL + 16384, FL_P = WS_FL + 24576, FL_EPOCH = WS_FL + 32768;
constexpr unsigned WS_USED = 19u << 20;
constexpr int LDS_BYTES = 163840;

DI unsigned pk_bf16(float lo, float hi) { f32x2 p = {lo, hi}; return __builtin_bit_cast(unsigned, __builtin_convertvector(p, bfx2)); }
DI float bf_lo(unsigned u) { return __uint_as_float(u << 16); }
DI float bf_hi(unsigned u) { return __uint_as_float(u & 0xffff0000u); }
DI float rbf(float x) { return bf_lo(pk_bf16(x, 0.f)); }
DI f32x16 mfma32(bf16x8 a, bf16x8 b, f32x16 c) { return __builtin_amdgcn_mfma_f32_32x32x16_bf16(a, b, c, 0, 0, 0); }
DI f32x4 mfma16(bf16x8 a, bf16x8 b, f32x4 c) { return __builtin_amdgcn_mfma_f32_16x16x32_bf16(a, b, c, 0, 0, 0); }
DI s16x4 tr_read(const char* p) { return __builtin_amdgcn_ds_read_tr16_b64_v4i16((LAS s16x4*)p); }
DI bf16x8 cat8(s16x4 a, s16x4 b) { return __builtin_shufflevector(a, b, 0, 1, 2, 3, 4, 5, 6, 7); }
DI rsrc_t mk_rsrc(const void* p, unsigned bytes) { return __builtin_amdgcn_make_buffer_rsrc((void*)p, 0, bytes, 0x00020000); }
DI void blds16(rsrc_t r, unsigned voff, unsigned soff, void* l) { __builtin_amdgcn_raw_ptr_buffer_load_lds(r, (LAS void*)l, 16, voff, soff, 0, 0); }
DI u32x4 ld16_sc1(rsrc_t r, unsigned voff, unsigned soff) { return __builtin_amdgcn_raw_buffer_load_b128(r, voff, soff, 16); }
DI void st16_sc1(u32x4 v, rsrc_t r, unsigned voff, unsigned soff) { __builtin_amdgcn_raw_buffer_store_b128(v, r, voff + soff, 0, 16); }
#define OUTST(v, off) __builtin_amdgcn_raw_buffer_store_b128(v, mk_rsrc(out, 2048 * 2048 * 2), off, 0, 17)
#define WAIT_V(n) asm volatile("s_waitcnt vmcnt(%0)" ::"n"(n) : "memory")
#define WAIT_L0() asm volatile("s_waitcnt lgkmcnt(0)" ::: "memory")
#define BAR() do { WAIT_L0(); __builtin_amdgcn_s_barrier(); asm volatile("" ::: "memory"); } while (0)
#define SCHED0() __builtin_amdgcn_sched_barrier(0)
#define SGB(m, n) __builtin_amdgcn_sched_group_barrier(m, n, 0)
template <int N> using IC = std::integral_constant<int, N>;
using T_ = std::true_type; using F_ = std::false_type;

DI bf16x8 pack8(const f32x16& x, int s) {
  u32x4 p;
  p[0] = pk_bf16(x[8 * s + 0], x[8 * s + 1]); p[1] = pk_bf16(x[8 * s + 2], x[8 * s + 3]);
  p[2] = pk_bf16(x[8 * s + 4], x[8 * s + 5]); p[3] = pk_bf16(x[8 * s + 6], x[8 * s + 7]);
  return __builtin_bit_cast(bf16x8, p);
}
DI u64 flag_value(unsigned epoch) { const unsigned t = epoch + 1u; return ((u64)t << 32) | (u64)(t ^ 0x5bd1e995u); }
DI void publish(char* ws, unsigned flag_off, u64 fv, int lane) {
  WAIT_V(0);
  if (lane == 0) __hip_atomic_store((GAS u64*)(ws + flag_off), fv, __ATOMIC_RELAXED, __HIP_MEMORY_SCOPE_AGENT);
}
DI void poll(const char* ws, unsigned flag_off, int n, u64 fv, int lane) {
  const GAS u64* p = (const GAS u64*)(ws + flag_off) + (lane < n ? lane : 0);
#pragma unroll 1
  for (unsigned spins = 0; spins < (1u << 20); ++spins) {
    const u64 v = __hip_atomic_load(p, __ATOMIC_RELAXED, __HIP_MEMORY_SCOPE_AGENT);
    if (__all(v == fv)) break;
    __builtin_amdgcn_s_sleep(2);
  }
  __builtin_amdgcn_fence(__ATOMIC_ACQUIRE, "wavefront");
  asm volatile("" ::: "memory");
}

DI f32x2 both_halves(float v) {
  const auto r = __builtin_amdgcn_permlane32_swap(__float_as_uint(v), __float_as_uint(v), false, false);
  return f32x2{__uint_as_float(r[0]), __uint_as_float(r[1])};
}
DI void dual_softmax(const f32x16& pin, bf16x8* pos, bf16x8* neg) {
  f32x2 p[8];
#pragma unroll
  for (int i = 0; i < 8; ++i) { const unsigned u = pk_bf16(pin[2 * i], pin[2 * i + 1]); p[i] = f32x2{bf_lo(u), bf_hi(u)}; }
  float mx = fmaxf(p[0][0], p[0][1]), mn = fminf(p[0][0], p[0][1]);
#pragma unroll
  for (int i = 1; i < 8; ++i) { mx = fmaxf(fmaxf(mx, p[i][0]), p[i][1]); mn = fminf(fminf(mn, p[i][0]), p[i][1]); }
  { const f32x2 a = both_halves(mx), b = both_halves(mn); mx = fmaxf(a[0], a[1]); mn = fminf(b[0], b[1]); }
  const float L2E = 1.4426950408889634f;
  const f32x2 l2 = {L2E, L2E}, nl2 = {-L2E, -L2E}, cp = {-mx * L2E, -mx * L2E}, cn = {mn * L2E, mn * L2E};
  f32x2 ep[8], en[8], sp = {0.f, 0.f}, sn = {0.f, 0.f};
#pragma unroll
  for (int i = 0; i < 8; ++i) {
    const f32x2 ap = __builtin_elementwise_fma(p[i], l2, cp), an = __builtin_elementwise_fma(p[i], nl2, cn);
    ep[i] = f32x2{__builtin_amdgcn_exp2f(ap[0]), __builtin_amdgcn_exp2f(ap[1])};
    en[i] = f32x2{__builtin_amdgcn_exp2f(an[0]), __builtin_amdgcn_exp2f(an[1])};
    sp += ep[i]; sn += en[i];
  }
  const f32x2 a = both_halves(sp[0] + sp[1]), b = both_halves(sn[0] + sn[1]);
  const float rp = __builtin_amdgcn_rcpf(a[0] + a[1]), rn = __builtin_amdgcn_rcpf(b[0] + b[1]);
  const f32x2 rp2 = {rp, rp}, rn2 = {rn, rn};
#pragma unroll
  for (int s = 0; s < 2; ++s) {
    u32x4 ua, ub;
#pragma unroll
    for (int i = 0; i < 4; ++i) {
      const f32x2 x = ep[4 * s + i] * rp2, y = en[4 * s + i] * rn2;
      ua[i] = pk_bf16(x[0], x[1]); ub[i] = pk_bf16(y[0], y[1]);
    }
    pos[s] = __builtin_bit_cast(bf16x8, ua); neg[s] = __builtin_bit_cast(bf16x8, ub);
  }
}

constexpr int G1_STAGE = 45056;
constexpr int CV_WAVE = 20480;
constexpr int L_E1 = 81920;
constexpr int L_KVF = 131072;
constexpr int L_WPHI = 135168;
constexpr int L_CNT = 137216;
constexpr int L_OUT = 131072;
constexpr int L_A3 = 147456;
DI int win_addr(int m0) {
  return ((m0 >> 3) << 6) + ((((m0 >> 1) & 3) ^ ((m0 >> 4) & 3)) << 4) + ((m0 & 1) << 3);
}
#define HBAR() do { __builtin_amdgcn_s_barrier(); asm volatile("" ::: "memory"); SCHED0(); } while (0)

DI void role_h(int b, int wave, char* sm, const u16* __restrict__ q, const u16* __restrict__ k, const u16* __restrict__ v,
               const u16* __restrict__ shk, const u16* __restrict__ hk, const u16* __restrict__ Dv, const u16* __restrict__ x,
               const u16* __restrict__ W, const u16* __restrict__ wo, char* ws, u16* __restrict__ out) {
  const int lane = threadIdx.x & 63;
  const int xcd = b & 7, a = xcd * 32 + (b >> 3), grp = a >> 4, idx = a & 15;
  const rsrc_t rw = mk_rsrc(ws, WS_USED);
  SCHED0();
  const unsigned epoch = __hip_atomic_load((GAS const unsigned*)(ws + FL_EPOCH), __ATOMIC_RELAXED, __HIP_MEMORY_SCOPE_AGENT);
  const int cgp = lane & 7, rg = lane >> 3;
  const int ch0 = grp * 64 + cgp * 8, r0 = idx * 128 + wave * 32 + rg * 4;
  const int ch = grp * 64 + idx * 4 + wave;
  u32x4 hv[4]; u32x2 hp[4];
  const rsrc_t rt = (wave & 1) ? mk_rsrc(W, 1024 * 3072 * 2) : mk_rsrc(x, 2048 * 1024 * 2);
  unsigned tvo;
  {
    const int bi = b >> 3, l48 = lane < 48 ? lane : 0;
    tvo = (wave & 1) ? ((bi * 4 + l48 / 12) * 3072 + ((l48 % 12) >> 2) * 1024 + (l48 & 3) * 64 + (xcd >> 1) * 256) * 2
                     : (((xcd & 1) * 1024 + bi * 32 + (lane >> 1)) * 1024 + (lane & 1) * 64) * 2;
  }
  const unsigned tstep = (wave & 1) ? 128 * 3072 * 2 : 256;
#define TD 6
  auto touch = [&](int t) {
    if (!(t & 1) && t + TD < 16 && ((t >> 1) & 1) == (wave >> 1))
      __builtin_amdgcn_raw_ptr_buffer_load_lds(rt, (LAS void*)(sm + L_CNT + 1024 + wave * 256), 4, tvo, ((t + TD) >> 1) * tstep, 0, 0);
  };
  touch(-4); touch(-2);
  SCHED0();
  HBAR();
  u32x4 kk[7], vv[4], sk[4];
  auto ldk = [&](int i) { const int r = r0 - 3 + i; kk[i] = *(const u32x4*)(k + (size_t)(r < 0 ? 0 : r) * 1024 + ch0); };
  auto ldv = [&](int i) { vv[i] = *(const u32x4*)(v + (size_t)(r0 + i) * 1024 + ch0); };
  auto lds_ = [&](int i) { sk[i] = *(const u32x4*)(shk + ch0 * 4 + i * 8); };
  const u16* hrow = hk + (size_t)ch * 2048;
  auto ldh = [&](int bb) { const int e0 = bb * 512 + lane * 8; hv[bb] = *(const u32x4*)(hrow + e0); hp[bb] = *(const u32x2*)(hrow + (e0 > 0 ? e0 - 4 : 0)); };
  ldk(0); ldk(1); ldk(2); touch(0); HBAR();
  ldk(3); ldk(4); ldk(5); HBAR();
  ldk(6); ldv(0); ldv(1); touch(2); HBAR();
  ldv(2); ldv(3); lds_(0); HBAR();
  lds_(1); lds_(2); lds_(3); touch(4); HBAR();
  ldh(0); HBAR();
  char* timg = sm + L_A3;
#pragma unroll
  for (int i = 0; i < 3; ++i) if (r0 - 3 + i < 0) kk[i] = u32x4{0u, 0u, 0u, 0u};
#pragma unroll
  for (int e2 = 0; e2 < 4; ++e2) {
    f32x2 w[4];
    w[0] = f32x2{bf_lo(sk[e2][0]), bf_lo(sk[e2][2])}; w[1] = f32x2{bf_hi(sk[e2][0]), bf_hi(sk[e2][2])};
    w[2] = f32x2{bf_lo(sk[e2][1]), bf_lo(sk[e2][3])}; w[3] = f32x2{bf_hi(sk[e2][1]), bf_hi(sk[e2][3])};
    f32x2 kf[7], o[4];
#pragma unroll
    for (int i = 0; i < 7; ++i) kf[i] = f32x2{bf_lo(kk[i][e2]), bf_hi(kk[i][e2])};
#pragma unroll
    for (int i = 0; i < 4; ++i) {
      f32x2 acc = kf[i] * w[3];
      acc = __builtin_elementwise_fma(kf[i + 1], w[2], acc);
      acc = __builtin_elementwise_fma(kf[i + 2], w[1], acc);
      acc = __builtin_elementwise_fma(kf[i + 3], w[0], acc);
      const unsigned ks = pk_bf16(acc[0], acc[1]);
      o[i] = f32x2{bf_lo(ks), bf_hi(ks)} * f32x2{bf_lo(vv[i][e2]), bf_hi(vv[i][e2])};
    }
    u32x2 wa = {pk_bf16(o[0][0], o[1][0]), pk_bf16(o[2][0], o[3][0])}, wb = {pk_bf16(o[0][1], o[1][1]), pk_bf16(o[2][1], o[3][1])};
    *(u32x2*)(timg + (cgp * 8 + 2 * e2) * 256 + (((wave * 8 + rg) ^ (2 * cgp)) << 3)) = wa;
    *(u32x2*)(timg + (cgp * 8 + 2 * e2 + 1) * 256 + (((wave * 8 + rg) ^ (2 * cgp)) << 3)) = wb;
    SCHED0();
    if (e2 < 3) ldh(e2 + 1);
    touch(6 + e2);
    if (e2 == 3) WAIT_L0();
    HBAR();
  }
  const unsigned dvraw = Dv[ch];
#pragma unroll
  for (int it = 0; it < 4; ++it) {
    const int c = wave * 16 + it * 4 + (lane >> 4), ck = lane & 15;
    const u32x4 d = *(const u32x4*)(timg + c * 256 + ((ck ^ (c >> 3)) << 4));
    st16_sc1(d, rw, (grp * 64 + c) * 4096 + idx * 256 + ck * 16, WS_XT);
    if (it == 1) HBAR();
  }
  SCHED0();
  HBAR(); HBAR(); HBAR();
  const u64 fv = flag_value(__builtin_amdgcn_readfirstlane(epoch));
  publish(ws, FL_A1 + (grp * 64 + idx * 4 + wave) * 8, fv, lane);
  SCHED0();
  HBAR(); HBAR();
  SCHED0();
  char* wl = sm + wave * CV_WAVE;
  char* xl = wl + 16384;
  const float dcoef = bf_lo(dvraw);
  if (lane == 0) { hp[0][0] = 0u; hp[0][1] = 0u; }
#pragma unroll
  for (int bb = 0; bb < 4; ++bb) {
    const unsigned dm2 = hp[bb][0], dm1 = hp[bb][1], d0 = hv[bb][0], d1 = hv[bb][1], d2 = hv[bb][2], d3 = hv[bb][3];
    unsigned R[10];
    R[0] = __builtin_amdgcn_perm(dm2, dm1, 0x07060100u);
    R[1] = __builtin_amdgcn_alignbit(dm1, dm1, 16);
    R[2] = __builtin_amdgcn_perm(dm1, d0, 0x07060100u);
    R[3] = __builtin_amdgcn_alignbit(d0, d0, 16);
    R[4] = __builtin_amdgcn_perm(d0, d1, 0x07060100u);
    R[5] = __builtin_amdgcn_alignbit(d1, d1, 16);
    R[6] = __builtin_amdgcn_perm(d1, d2, 0x07060100u);
    R[7] = __builtin_amdgcn_alignbit(d2, d2, 16);
    R[8] = __builtin_amdgcn_perm(d2, d3, 0x07060100u);
    R[9] = __builtin_amdgcn_alignbit(d3, d3, 16);
    const int i = bb * 64 + lane, sw = (i >> 1) & 3;
#pragma unroll
    for (int r = 0; r < 4; ++r) {
      u32x4 w = {R[2 * r + 2], R[2 * r], R[2 * r + 3], R[2 * r + 1]};
      *(u32x4*)(wl + i * 64 + ((r ^ sw) << 4)) = w;
    }
  }
  SCHED0();
  poll(ws, FL_A1 + grp * 512, 64, fv, lane);
  SCHED0();
  u32x4 xv[4];
#pragma unroll
  for (int bb = 0; bb < 4; ++bb) xv[bb] = ld16_sc1(rw, ch * 4096 + bb * 1024 + lane * 16, WS_XT);
  SCHED0();
  {
    const int wv = (b >> 3) * 4 + wave;
    __builtin_amdgcn_raw_ptr_buffer_load_lds(mk_rsrc(wo, 1024 * 1024 * 2), (LAS void*)(sm + L_CNT + 1024 + wave * 256), 4,
                                             ((wv * 8 + (lane >> 3)) * 1024 + (xcd >> 2) * 512 + (lane & 7) * 64) * 2, 0, 0, 0);
    __builtin_amdgcn_raw_ptr_buffer_load_lds(mk_rsrc(q, 2048 * 1024 * 2), (LAS void*)(sm + L_CNT + 1024 + wave * 256), 4,
                                             ((idx * 128 + wave * 32 + (lane & 31)) * 1024 + grp * 64) * 2, 0, 0, 0);
  }
  SCHED0();
#pragma unroll
  for (int bb = 0; bb < 4; ++bb) *(u32x4*)(xl + (lane & 1) * 2048 + (bb * 32 + (lane >> 1)) * 16) = xv[bb];
  SCHED0();
  const int ca = lane & 15, g = lane >> 4;
  const int off = (g & 1) * 16 + (g >> 1) * 8;
  const int cb = 2 * ca + 1 - (off >> 3);
  const int xb0 = 16384 + (cb & 1) * 2048 + ((cb >> 1) << 4);
  f32x4 acc[8];
#pragma unroll
  for (int c = 0; c < 8; ++c) acc[c] = f32x4{0.f, 0.f, 0.f, 0.f};
  bf16x8 A[2][8], B[2][8];
  const bf16x8 zero8 = {0, 0, 0, 0, 0, 0, 0, 0};
  const int wbase = wave * CV_WAVE;
  auto ld = [&](int set, int t0, auto FIRST) {
    const int m0 = 32 * t0 + ca + off - 8;
    const char* pb = wl + xb0 - t0 * 32;
    if constexpr (FIRST.value) {
      const s16x4 z4 = {0, 0, 0, 0};
      s16x4 a0 = *(const s16x4*)(wl + win_addr(m0 < 0 ? 0 : m0)), a1 = *(const s16x4*)(wl + win_addr(m0 - 4 < 0 ? 0 : m0 - 4));
      A[set][0] = cat8(m0 < 0 ? z4 : a0, m0 - 4 < 0 ? z4 : a1);
    }
    const int pa0 = wbase + win_addr(m0 + 256) - 2048, pa1 = wbase + win_addr(m0 + 252) - 2048;
#pragma unroll
    for (int i = 0; i < 8; ++i) {
      B[set][7 - i] = *(const bf16x8*)(pb + (7 - i) * 256);
      if (!(FIRST.value && i == 0)) {
        int q0 = pa0 + i * 2048, q1 = pa1 + i * 2048;
        asm volatile("" : "+v"(q0), "+v"(q1));
        A[set][i] = cat8(*(const s16x4*)(sm + q0), *(const s16x4*)(sm + q1));
      }
    }
  };
  auto mm = [&](int set, int t0) {
    const bf16x8 b0 = (cb - 4 * t0 < 0) ? zero8 : B[set][0];
#pragma unroll
    for (int t1 = 0; t1 < 8; ++t1)
#pragma unroll
      for (int i = 7 - t1; i >= 0; --i) acc[t1 + i] = mfma16(A[set][t1], i == 0 ? b0 : B[set][i], acc[t1 + i]);
  };
  __builtin_amdgcn_s_waitcnt(0xC07F);
  ld(0, 0, T_{});
  SCHED0();
#define CV_SCHED() do { _Pragma("unroll") for (int z = 0; z < 24; ++z) { SGB(0x8, 1); SGB(0x100, 1); } SGB(0x8, 12); SCHED0(); \
    asm volatile("" : "+v"(acc[0]), "+v"(acc[1]), "+v"(acc[2]), "+v"(acc[3]), "+v"(acc[4]), "+v"(acc[5]), "+v"(acc[6]), "+v"(acc[7])); } while (0)
#pragma unroll 1
  for (int t0 = 0; t0 < 6; t0 += 2) {
    ld(1, t0 + 1, F_{}); mm(0, t0); CV_SCHED();
    ld(0, t0 + 2, F_{}); mm(1, t0 + 1); CV_SCHED();
  }
  ld(1, 7, F_{}); mm(0, 6); CV_SCHED();
  mm(1, 7);
  SCHED0();
  const rsrc_t ro = mk_rsrc(wo, 1024 * 1024 * 2);
  const unsigned wosrc = ((wave * 160 + ((lane >> 2) & 7)) * 1024 + ((xcd >> 2) * 8 + (b >> 5)) * 64 + (lane >> 5) * 32 + (lane & 3) * 8) * 2;
  WAIT_L0();
#pragma unroll
  for (int p = 0; p < 16; ++p) blds16(ro, wosrc, p * 16384, wl + p * 1024);
  SCHED0();
  char* yl = sm + L_A3 + wave * 4096;
  {
    const int xe = 16384 + (g >> 1) * 2048 + (ca << 4) + (g & 1) * 8;
#pragma unroll
    for (int c = 0; c < 8; ++c) {
      const u32x2 kv = *(const u32x2*)(wl + xe + c * 256);
      const unsigned y01 = pk_bf16(acc[c][0], acc[c][1]), y23 = pk_bf16(acc[c][2], acc[c][3]);
      const unsigned t01 = pk_bf16(bf_lo(kv[0]) * dcoef, bf_hi(kv[0]) * dcoef), t23 = pk_bf16(bf_lo(kv[1]) * dcoef, bf_hi(kv[1]) * dcoef);
      u32x2 o = {pk_bf16(bf_lo(y01) + bf_lo(t01), bf_hi(y01) + bf_hi(t01)), pk_bf16(bf_lo(y23) + bf_lo(t23), bf_hi(y23) + bf_hi(t23))};
      *(u32x2*)(yl + c * 512 + ca * 32 + g * 8) = o;
    }
#pragma unroll
    for (int it = 0; it < 4; ++it) st16_sc1(*(const u32x4*)(yl + it * 1024 + lane * 16), rw, ch * 4096 + it * 1024 + lane * 16, WS_YT);
  }
  publish(ws, FL_Y + (grp * 64 + idx * 4 + wave) * 8, fv, lane);
  SCHED0();
  WAIT_L0();
#pragma unroll
  for (int p = 16; p < 20; ++p) blds16(ro, wosrc, p * 16384, wl + p * 1024);
  WAIT_V(0);
  u32x4 qq[4];
#pragma unroll
  for (int i = 0; i < 4; ++i) qq[i] = *(const u32x4*)(q + (size_t)(r0 + i) * 1024 + ch0);
  SCHED0();
  HBAR();
  poll(ws, FL_Y + grp * 512, 64, fv, lane);
  SCHED0();
  {
    u32x4 yy[4];
#pragma unroll
    for (int it = 0; it < 4; ++it) {
      const int c = it * 16 + (lane >> 2), ck = lane & 3;
      yy[it] = ld16_sc1(rw, (grp * 64 + c) * 4096 + (idx * 128 + wave * 32 + ck * 8) * 2, WS_YT);
    }
#pragma unroll
    for (int it = 0; it < 4; ++it) {
      const int c = it * 16 + (lane >> 2), ck = lane & 3;
      *(u32x4*)(yl + c * 64 + ((ck ^ ((c >> 4) & 3)) << 4)) = yy[it];
    }
    u32x2 y[8];
#pragma unroll
    for (int e = 0; e < 8; ++e) y[e] = *(const u32x2*)(yl + (cgp * 8 + e) * 64 + ((rg ^ (cgp & 6)) << 3));
#pragma unroll
    for (int i = 0; i < 4; ++i) {
      u32x4 o;
#pragma unroll
      for (int e2 = 0; e2 < 4; ++e2) {
        const unsigned ya = y[2 * e2][i >> 1], yb = y[2 * e2 + 1][i >> 1];
        const float fa = (i & 1) ? bf_hi(ya) : bf_lo(ya), fb = (i & 1) ? bf_hi(yb) : bf_lo(yb);
        o[e2] = pk_bf16(bf_lo(qq[i][e2]) * fa, bf_hi(qq[i][e2]) * fb);
      }
      OUTST(o, ((r0 + i) * 2048 + ch0) * 2);
    }
  }
  SCHED0();
}

DI void lds_sync(LAS unsigned* cnt, unsigned target, int lane) {
  WAIT_L0();
  if (lane == 0) __hip_atomic_fetch_add(cnt, 1u, __ATOMIC_RELAXED, __HIP_MEMORY_SCOPE_WORKGROUP);
#pragma unroll 1
  for (unsigned spins = 0; spins < (1u << 24); ++spins) {
    if (__hip_atomic_load(cnt, __ATOMIC_RELAXED, __HIP_MEMORY_SCOPE_WORKGROUP) >= target) break;
    __builtin_amdgcn_s_sleep(1);
  }
  asm volatile("" ::: "memory");
}

DI void role_g(int bid, int wave, char* sm, const u16* __restrict__ x, const u16* __restrict__ W, const u16* __restrict__ wphi,
               const u16* __restrict__ wo, char* ws, u16* __restrict__ out) {
  int lane = threadIdx.x & 63;
  const int xcd = bid & 7, idx = bid >> 3;
  const int rb = (xcd & 1) * 4 + (idx & 3), head = (xcd >> 1) * 8 + (idx >> 2);
  const int rb3 = (xcd & 3) * 4 + (idx & 3), nb = (xcd >> 2) * 8 + (idx >> 2);
  int h = lane >> 5, l31 = lane & 31;
  int blk = (lane >> 4) & 1, q4 = (lane & 15) >> 2, p4 = lane & 3;
  int troff = q4 * 64 + (2 * blk + (p4 >> 1)) * 16 + (p4 & 1) * 8;
#define RELANE() do { asm volatile("" : "+v"(lane)); h = lane >> 5; l31 = lane & 31; blk = (lane >> 4) & 1; q4 = (lane & 15) >> 2; p4 = lane & 3; \
    troff = q4 * 64 + (2 * blk + (p4 >> 1)) * 16 + (p4 & 1) * 8; SCHED0(); } while (0)
  const rsrc_t rx = mk_rsrc(x, 2048 * 1024 * 2), rW = mk_rsrc(W, 1024 * 3072 * 2), rp = mk_rsrc(wphi, 65536), rw = mk_rsrc(ws, WS_USED);
  SCHED0();
  f32x16 zero;
#pragma unroll
  for (int i = 0; i < 16; ++i) zero[i] = 0.f;
  LAS unsigned* cnt = (LAS unsigned*)(sm + L_CNT);
  if (wave == 0 && lane < 2) cnt[lane] = 0u;
  f32x16 acc[2][3];
  {
    unsigned xs[2], wsrc;
#pragma unroll
    for (int i = 0; i < 2; ++i) {
      const int row = (wave * 8 + i) * 8 + (lane >> 3), c = (lane & 7) ^ ((row >> 1) & 7);
      xs[i] = ((rb * 256 + row) * 1024 + c * 8) * 2;
    }
    wsrc = (((lane >> 5) * 8 + ((lane >> 2) & 7)) * 3072 + head * 32 + (lane & 3) * 8) * 2;
    auto piece = [&](int t, int buf, int i) {
      char* bb = sm + buf * G1_STAGE;
      if (i < 8) blds16(rx, xs[i & 1] + (i >> 1) * 32768, t * 128, bb + (wave * 8 + i) * 1024);
      else { const int p = wave * 3 + i - 8; blds16(rW, wsrc, t * (64 * 3072 * 2) + ((p & 3) * 16 * 3072 + (p >> 2) * 1024) * 2, bb + 32768 + p * 1024); }
    };
#pragma unroll
    for (int i = 0; i < 11; ++i) piece(0, 0, i);
    if (wave < 2) blds16(rp, (head * 1024 + wave * 512 + lane * 8) * 2, 0, sm + L_WPHI + wave * 1024);
#pragma unroll
    for (int i = 0; i < 11; ++i) piece(1, 1, i);
#pragma unroll
    for (int i = 0; i < 4; ++i) piece(2, 2, i);
    int xoff[4];
    {
      const int row = wave * 64 + l31, sw = (row >> 1) & 7;
#pragma unroll
      for (int s = 0; s < 4; ++s) xoff[s] = row * 128 + (((2 * s + h) ^ sw) << 4);
    }
    const int woff = 32768 + h * 512 + troff;
#pragma unroll
    for (int a2 = 0; a2 < 2; ++a2)
#pragma unroll
      for (int b2 = 0; b2 < 3; ++b2) acc[a2][b2] = zero;
    bf16x8 xf[2][2], wf[2][3];
    auto rd = [&](int set, const char* bb, int s) {
      xf[set][0] = *(const bf16x8*)(bb + xoff[s]);
#pragma unroll
      for (int gq = 0; gq < 3; ++gq) wf[set][gq] = cat8(tr_read(bb + woff + s * 1024 + gq * 4096), tr_read(bb + woff + s * 1024 + gq * 4096 + 256));
      xf[set][1] = *(const bf16x8*)(bb + xoff[s] + 4096);
    };
    auto mm = [&](int set) {
#pragma unroll
      for (int ts = 0; ts < 2; ++ts) {
        acc[ts][0] = mfma32(wf[set][0], xf[set][ts], acc[ts][0]);
        acc[ts][1] = mfma32(wf[set][1], xf[set][ts], acc[ts][1]);
        acc[ts][2] = mfma32(xf[set][ts], wf[set][2], acc[ts][2]);
      }
    };
    auto pin = [&]() { asm volatile("" : "+v"(acc[0][0]), "+v"(acc[0][1]), "+v"(acc[0][2]), "+v"(acc[1][0]), "+v"(acc[1][1]), "+v"(acc[1][2])); };
#define G1_SCHED(NV) do { SGB(0x100, 3); SGB(0x8, 1); SGB(0x100, 2); SGB(0x8, 1); SGB(0x100, 3); SGB(0x8, 1); \
    if ((NV) > 0) SGB(0x10, 1); SGB(0x8, 1); if ((NV) > 1) SGB(0x10, 1); SGB(0x8, 1); if ((NV) > 2) SGB(0x10, 1); SGB(0x8, 1); if ((NV) > 3) SGB(0x10, 1); SCHED0(); } while (0)
    SCHED0();
    WAIT_V(15); BAR();
    SCHED0();
    __builtin_amdgcn_s_waitcnt(0xC07F);
    rd(0, sm, 0);
    SCHED0();
    int cur = 0, nxt = 1, nx2 = 2;
    auto step = [&](int t, auto BC, auto AA, auto WN, auto RN) {
      const char* bb = sm + cur * G1_STAGE;
      rd(1, bb, 1);
      if constexpr (BC.value) { piece(t + 2, nx2, 4); piece(t + 2, nx2, 5); piece(t + 2, nx2, 6); piece(t + 2, nx2, 7); }
      mm(0); G1_SCHED(BC.value ? 4 : 0); pin();
      rd(0, bb, 2);
      if constexpr (BC.value) { piece(t + 2, nx2, 8); piece(t + 2, nx2, 9); piece(t + 2, nx2, 10); }
      mm(1); G1_SCHED(BC.value ? 3 : 0); pin();
      rd(1, bb, 3); mm(0); G1_SCHED(0); pin();
      WAIT_V(WN.value);
      BAR();
      SCHED0();
      if constexpr (RN.value) rd(0, sm + nxt * G1_STAGE, 0);
      if constexpr (AA.value) { piece(t + 3, cur, 0); piece(t + 3, cur, 1); piece(t + 3, cur, 2); piece(t + 3, cur, 3); }
      mm(1); G1_SCHED(AA.value ? 4 : 0); pin();
      const int o = cur; cur = nxt; nxt = nx2; nx2 = o;
    };
#pragma unroll 1
    for (int t = 0; t < 13; ++t) step(t, T_{}, T_{}, IC<11>{}, T_{});
    step(13, T_{}, F_{}, IC<11>{}, T_{});
    step(14, F_{}, F_{}, IC<0>{}, T_{});
    step(15, F_{}, F_{}, IC<0>{}, F_{});
  }
  SCHED0();
  RELANE();
  const unsigned epoch = __hip_atomic_load((GAS const unsigned*)(ws + FL_EPOCH), __ATOMIC_RELAXED, __HIP_MEMORY_SCOPE_AGENT);
  bf16x8 wp[2];
  {
    const char* wb = sm + L_WPHI + h * 256 + troff;
#pragma unroll
    for (int s = 0; s < 2; ++s) wp[s] = cat8(tr_read(wb + s * 1024), tr_read(wb + s * 1024 + 512));
  }
  u64 fv;
  {
    char* img = sm + L_E1 + wave * 8192;
#pragma unroll
    for (int ts = 0; ts < 2; ++ts) {
      f32x16 pk = mfma32(wp[0], pack8(acc[ts][1], 0), zero);
      pk = mfma32(wp[1], pack8(acc[ts][1], 1), pk);
      bf16x8 pos[2], neg[2];
      dual_softmax(pk, pos, neg);
      const int row = ts * 32 + l31, sw = (row >> 1) & 7;
#pragma unroll
      for (int gq = 0; gq < 4; ++gq) {
        const u32x4 pp = __builtin_bit_cast(u32x4, pos[gq >> 1]), nn = __builtin_bit_cast(u32x4, neg[gq >> 1]);
        u32x2 aa = {pp[(gq & 1) * 2], pp[(gq & 1) * 2 + 1]}, bb = {nn[(gq & 1) * 2], nn[(gq & 1) * 2 + 1]};
        const int o = row * 64 + (((2 * gq + h) ^ sw) << 3);
        *(u32x2*)(img + o) = aa;
        *(u32x2*)(img + 4096 + o) = bb;
      }
    }
    f32x16 kvacc[2], zacc[2];
    kvacc[0] = zero; kvacc[1] = zero; zacc[0] = zero; zacc[1] = zero;
    bf16x8 ones;
#pragma unroll
    for (int i = 0; i < 8; ++i) ones[i] = (short)0x3f80;
#pragma unroll
    for (int ts = 0; ts < 2; ++ts)
#pragma unroll
      for (int s = 0; s < 2; ++s) {
        const bf16x8 vb = pack8(acc[ts][2], s);
        const int r0 = ts * 32 + 16 * s + 4 * h + q4, r1 = r0 + 8;
        const int o0 = r0 * 64 + (((4 * blk + p4) ^ ((r0 >> 1) & 7)) << 3), o1 = r1 * 64 + (((4 * blk + p4) ^ ((r1 >> 1) & 7)) << 3);
#pragma unroll
        for (int tl = 0; tl < 2; ++tl) {
          const bf16x8 pa = cat8(tr_read(img + tl * 4096 + o0), tr_read(img + tl * 4096 + o1));
          kvacc[tl] = mfma32(pa, vb, kvacc[tl]);
          zacc[tl] = mfma32(pa, ones, zacc[tl]);
        }
      }
    fv = flag_value(__builtin_amdgcn_readfirstlane(epoch));
    asm volatile("" ::"s"(fv));
    char* zred = sm + L_E1 + 32768;
#pragma unroll
    for (int tl = 0; tl < 2; ++tl)
#pragma unroll
      for (int qd = 0; qd < 4; ++qd) {
        f32x4 t = {kvacc[tl][4 * qd], kvacc[tl][4 * qd + 1], kvacc[tl][4 * qd + 2], kvacc[tl][4 * qd + 3]};
        *(f32x4*)(img + (tl * 4 + qd) * 1024 + lane * 16) = t;
      }
    if (l31 == 0) {
#pragma unroll
      for (int tl = 0; tl < 2; ++tl)
#pragma unroll
        for (int qd = 0; qd < 4; ++qd) {
          f32x4 z = {zacc[tl][4 * qd], zacc[tl][4 * qd + 1], zacc[tl][4 * qd + 2], zacc[tl][4 * qd + 3]};
          *(f32x4*)(zred + wave * 256 + ((tl * 2 + h) * 4 + qd) * 16) = z;
        }
    }
    lds_sync(cnt, 4u, lane);
#pragma unroll
    for (int r = 0; r < 2; ++r) {
      const char* src = sm + L_E1 + (wave * 2 + r) * 1024 + lane * 16;
      f32x4 t = *(const f32x4*)src;
      t += *(const f32x4*)(src + 8192); t += *(const f32x4*)(src + 16384); t += *(const f32x4*)(src + 24576);
      st16_sc1(__builtin_bit_cast(u32x4, t), rw, ((rb * 32 + head) * 8 + wave * 2 + r) * 1024 + lane * 16, WS_KVP);
    }
    if (lane < 16) {
      const float* zs = (const float*)(zred + wave * 64 + lane * 4);
      const float z = (zs[0] + zs[64]) + (zs[128] + zs[192]);
      __builtin_amdgcn_raw_buffer_store_b32(__float_as_uint(z), rw, WS_ZP + (rb * 32 + head) * 256 + wave * 64 + lane * 4, 0, 16);
    }
  }
  SCHED0();
  bf16x8 ph[2][4];
  {
    f32x16 pq = mfma32(wp[0], pack8(acc[0][0], 0), zero);
    pq = mfma32(wp[1], pack8(acc[0][0], 1), pq);
    dual_softmax(pq, ph[0], ph[0] + 2);
  }
  SCHED0();
  publish(ws, FL_P + ((head * 4 + wave) * 8 + rb) * 8, fv, lane);
  SCHED0();
  {
    f32x16 pq = mfma32(wp[0], pack8(acc[1][0], 0), zero);
    pq = mfma32(wp[1], pack8(acc[1][0], 1), pq);
    dual_softmax(pq, ph[1], ph[1] + 2);
  }
  SCHED0();
  RELANE();
  {
    poll(ws, FL_P + (head * 4 + wave) * 64, 8, fv, lane);
    SCHED0();
    u32x4 e[16]; float ze[8];
#pragma unroll
    for (int r = 0; r < 8; ++r) {
      e[2 * r] = ld16_sc1(rw, (head * 8 + wave * 2) * 1024 + lane * 16, WS_KVP + r * 262144);
      e[2 * r + 1] = ld16_sc1(rw, (head * 8 + wave * 2 + 1) * 1024 + lane * 16, WS_KVP + r * 262144);
    }
#pragma unroll
    for (int r = 0; r < 8; ++r) ze[r] = __uint_as_float(__builtin_amdgcn_raw_buffer_load_b32(rw, head * 256 + wave * 64 + (lane & 15) * 4, WS_ZP + r * 8192, 16));
    f32x4 s0 = {0.f, 0.f, 0.f, 0.f}, s1 = s0; float zs = 0.f;
#pragma unroll
    for (int r = 0; r < 8; ++r) { s0 += __builtin_bit_cast(f32x4, e[2 * r]); s1 += __builtin_bit_cast(f32x4, e[2 * r + 1]); zs += ze[r]; }
    const u32x4 fr = {pk_bf16(s0[0], s0[1]), pk_bf16(s0[2], s0[3]), pk_bf16(s1[0], s1[1]), pk_bf16(s1[2], s1[3])};
    *(u32x4*)(sm + L_KVF + wave * 1024 + lane * 16) = fr;
    if (lane < 16) *(u16*)(sm + L_CNT + 64 + wave * 32 + lane * 2) = (u16)(pk_bf16(zs, 0.f) & 0xffffu);
    lds_sync(cnt + 1, 4u, lane);
  }
  SCHED0();
  {
    bf16x8 kvf[4], zf[4];
#pragma unroll
    for (int i = 0; i < 4; ++i) {
      kvf[i] = *(const bf16x8*)(sm + L_KVF + i * 1024 + lane * 16);
      zf[i] = *(const bf16x8*)(sm + L_CNT + 64 + ((i >> 1) * 2 + h) * 32 + (i & 1) * 16);
    }
    {
      const rsrc_t ro = mk_rsrc(wo, 1024 * 1024 * 2);
      const unsigned wosrc = (((80 + wave * 12) * 8 + ((lane >> 2) & 7)) * 1024 + nb * 64 + (lane >> 5) * 32 + (lane & 3) * 8) * 2;
#pragma unroll
      for (int p = 0; p < 12; ++p) blds16(ro, wosrc, p * 16384, sm + (80 + wave * 12 + p) * 1024);
    }
#pragma unroll
    for (int ts = 0; ts < 2; ++ts) {
      f32x16 num = zero, den = zero;
#pragma unroll
      for (int i = 0; i < 4; ++i) { num = mfma32(kvf[i], ph[ts][i], num); den = mfma32(zf[i], ph[ts][i], den); }
      const float dn = rbf(rbf(den[0]) + 1e-6f);
      const float rdn = 1.0f / dn;
      f32x16 o;
#pragma unroll
      for (int i = 0; i < 8; ++i) {
        const unsigned u = pk_bf16(num[2 * i], num[2 * i + 1]);
        o[2 * i] = bf_lo(u) * rdn; o[2 * i + 1] = bf_hi(u) * rdn;
      }
      const unsigned dst = (((rb * 8 + wave * 2 + ts) * 32 + head) * 2) * 1024 + lane * 16;
      st16_sc1(__builtin_bit_cast(u32x4, pack8(o, 0)), rw, dst, WS_OF);
      st16_sc1(__builtin_bit_cast(u32x4, pack8(o, 1)), rw, dst + 1024, WS_OF);
    }
    publish(ws, FL_O + ((rb * 4 + wave) * 32 + head) * 8, fv, lane);
  }
  SCHED0();
  {
    if (wave == 0) poll(ws, FL_O + rb3 * 512, 64, fv, lane);
    lds_sync(cnt, 8u, lane);
    SCHED0();
    const unsigned osrc = (rb3 * 4 + wave) * 65536 + lane * 16;
    bf16x8 of[16];
#pragma unroll
    for (int i = 0; i < 16; ++i) of[i] = __builtin_bit_cast(bf16x8, ld16_sc1(rw, osrc + i * 1024, WS_OF + 40 * 1024));
    f32x16 oa[2];
    oa[0] = zero; oa[1] = zero;
    bf16x8 wf[2][2];
    const char* wb = sm + h * 256 + troff;
    auto rd = [&](int set, const char* bb) {
      wf[set][0] = cat8(tr_read(bb), tr_read(bb + 1024));
      wf[set][1] = cat8(tr_read(bb + 512), tr_read(bb + 1536));
    };
    SCHED0();
    __builtin_amdgcn_s_waitcnt(0xC07F);
    rd(0, wb + 40 * 2048);
    SCHED0();
    auto chunk = [&](auto C) {
      constexpr int c = C.value;
#pragma unroll
      for (int i = 0; i < 16; ++i) {
        if (c == 1 && i == 7) { BAR(); SCHED0(); }
        if (!(c == 3 && i == 15)) rd((i + 1) & 1, wb + ((16 * c + i + 41) & 63) * 2048);
        oa[0] = mfma32(wf[i & 1][0], of[i], oa[0]);
        oa[1] = mfma32(wf[i & 1][1], of[i], oa[1]);
        if constexpr (c < 3) of[i] = __builtin_bit_cast(bf16x8, ld16_sc1(rw, osrc, WS_OF + ((16 * c + i + 56) & 63) * 1024));
        SGB(0x100, 2); SGB(0x8, 1); SGB(0x100, 2); SGB(0x8, 1); SGB(0x10, 1); SCHED0();
        asm volatile("" : "+v"(oa[0]), "+v"(oa[1]));
      }
    };
    chunk(IC<0>{}); chunk(IC<1>{}); chunk(IC<2>{}); chunk(IC<3>{});
    SCHED0();
    RELANE();
    char* ot = sm + L_OUT + wave * 4096;
#pragma unroll
    for (int gq = 0; gq < 2; ++gq)
#pragma unroll
      for (int qd = 0; qd < 4; ++qd) {
        u32x2 w = {pk_bf16(oa[gq][4 * qd], oa[gq][4 * qd + 1]), pk_bf16(oa[gq][4 * qd + 2], oa[gq][4 * qd + 3])};
        *(u32x2*)(ot + l31 * 128 + (((4 * gq + qd) ^ (l31 & 7)) << 4) + h * 8) = w;
      }
#pragma unroll
    for (int it = 0; it < 4; ++it) {
      const int row = it * 8 + (lane >> 3), c = lane & 7;
      const u32x4 d = *(const u32x4*)(ot + row * 128 + ((c ^ (row & 7)) << 4));
      OUTST(d, ((rb3 * 128 + wave * 32 + row) * 2048 + 1024 + nb * 64 + c * 8) * 2);
    }
  }
  if (bid == 0 && threadIdx.x == 0) __hip_atomic_store((GAS unsigned*)(ws + FL_EPOCH), epoch + 1u, __ATOMIC_RELAXED, __HIP_MEMORY_SCOPE_AGENT);
  SCHED0();
}

__global__ __launch_bounds__(512, 2) void fused_kernel(const u16* q, const u16* k, const u16* v, const u16* shk, const u16* hk, const u16* Dv,
                                                        const u16* x, const u16* W, const u16* wphi, const u16* wo, char* ws, u16* out) {
  extern __shared__ __attribute__((aligned(16))) char sm[];
  const int wave = __builtin_amdgcn_readfirstlane(threadIdx.x >> 6);
  if (wave < 4) role_g(blockIdx.x, wave, sm, x, W, wphi, wo, ws, out);
  else role_h(blockIdx.x, wave - 4, sm, q, k, v, shk, hk, Dv, x, W, wo, ws, out);
}


extern "C" void kernel_launch(void* const* d_in, const int* in_sizes, int n_in, void* d_out, int out_size, void* d_ws, size_t ws_size,
                              hipStream_t stream) {
  (void)hipFuncSetAttribute((const void*)fused_kernel, hipFuncAttributeMaxDynamicSharedMemorySize, LDS_BYTES);
  fused_kernel<<<256, 512, LDS_BYTES, stream>>>((const u16*)d_in[0], (const u16*)d_in[1], (const u16*)d_in[2], (const u16*)d_in[3],
                                                (const u16*)d_in[4], (const u16*)d_in[5], (const u16*)d_in[6], (const u16*)d_in[7],
                                                (const u16*)d_in[8], (const u16*)d_in[9], (char*)d_ws, (u16*)d_out);
}
```
